```python
import torch

def _softmax(x, axis=-1):
    return torch.softmax(x, dim=axis)
def _gelu(x):
    return torch.nn.functional.gelu(x, approximate="tanh")
def _silu(x):
    return torch.nn.functional.silu(x)
def _sigmoid(x):
    return torch.sigmoid(x)
def _layernorm(x, axis=-1, eps=1e-5):
    m = x.mean(dim=axis, keepdim=True)
    v = x.var(dim=axis, keepdim=True, unbiased=False)
    return (x - m) / torch.sqrt(v + eps)
def _scatter_add(tgt, idx, src, axis=0):
    return tgt.clone().index_add_(axis, idx, src)
def _segment_sum(src, seg, n):
    out = torch.zeros((n,) + src.shape[1:], dtype=src.dtype)
    return out.index_add_(0, seg, src)
def _pad(x, pw):
    flat = tuple(v for pair in reversed(pw) for v in pair)
    return torch.nn.functional.pad(x, flat)

kernel_name = 'standalone__detection_seg_dino__vision'

def setup_inputs():

    _w = (lambda a: (lambda t: t.to(torch.bfloat16) if t.is_floating_point() else t)(torch.as_tensor(a)))
    H = W = (384 // 2) * 2
    C = 96
    Nq = 16
    sc = C ** -0.5
    return {
        "q": _w((lambda *s: torch.randn(*s, dtype=torch.bfloat16) * 0.05)(1, Nq, C)),
        "f0": _w((lambda *s: torch.randn(*s, dtype=torch.bfloat16) * 0.8)(1, H, W, C)),
        "f1": _w((lambda *s: torch.randn(*s, dtype=torch.bfloat16) * 0.8)(1, H // 2, W // 2, C)),
        "wq": _w((lambda *s: torch.randn(*s, dtype=torch.bfloat16) * 3.2)(C, C) * sc),
        "wk": _w((lambda *s: torch.randn(*s, dtype=torch.bfloat16) * 3.2)(C, C) * sc),
        "wv": _w((lambda *s: torch.randn(*s, dtype=torch.bfloat16) * 0.8)(C, C) * sc),
        "wo": _w((lambda *s: torch.randn(*s, dtype=torch.bfloat16) * 0.8)(C, C) * sc),
    }

def reference(q, f0, f1, wq, wk, wv, wo):
    B, H, W, C = f0.shape
    f1u = (lambda x, n, axis: torch.repeat_interleave(x, n, dim=axis))((lambda x, n, axis: torch.repeat_interleave(x, n, dim=axis))(f1, 2, axis=1), 2, axis=2)
    fused = f0 + f1u
    feats = _layernorm(fused.reshape(B, H * W, C), axis=-1)
    qn = _layernorm(q, axis=-1)
    Q = torch.einsum("bnc,cd->bnd", qn, wq)
    K = torch.einsum("bmc,cd->bmd", feats, wk)
    V = torch.einsum("bmc,cd->bmd", feats, wv)
    scale = C ** -0.5
    logits = torch.einsum("bnd,bmd->bnm", Q, K) * scale
    attn = _softmax(logits, axis=-1)
    ctx = torch.einsum("bnm,bmd->bnd", attn, V)
    return q + torch.einsum("bnd,dc->bnc", ctx, wo)
```

```cpp
#include <hip/hip_runtime.h>
#include <hip/hip_bf16.h>
#define MIMIC 0
typedef unsigned short u16;
__device__ __forceinline__ float b2f(u16 x){ return __uint_as_float(((unsigned)x)<<16); }
__device__ __forceinline__ float rb(float x){ return __bfloat162float(__float2bfloat16(x)); }
constexpr int C=96, NQ=16, W=384, H=384, NPX=W*H, NB=NPX/256;
__global__ void prep(const u16* q,const u16* wq,const u16* wk,float* ws){
  __shared__ float qn[NQ*C], Q[NQ*C];
  int t=threadIdx.x;
  if(t<NQ){ float m=0; for(int c=0;c<C;c++) m+=b2f(q[t*C+c]); m/=C; float v=0; for(int c=0;c<C;c++){float d=b2f(q[t*C+c])-m; v+=d*d;} v/=C;
    if(MIMIC){ m=rb(m); v=rb(v); float s=rb(sqrtf(rb(v+1e-5f))); for(int c=0;c<C;c++) qn[t*C+c]=rb(rb(b2f(q[t*C+c])-m)/s);}
    else { float r=1.f/sqrtf(v+1e-5f); for(int c=0;c<C;c++) qn[t*C+c]=(b2f(q[t*C+c])-m)*r; } }
  __syncthreads();
  for(int i=t;i<NQ*C;i+=256){int n=i/C,d=i%C; float a=0; for(int c=0;c<C;c++) a+=qn[n*C+c]*b2f(wq[c*C+d]); Q[i]=MIMIC?rb(a):a; ws[i]=Q[i];}
  __syncthreads();
  for(int i=t;i<NQ*C;i+=256){int n=i/C,c=i%C; float a=0; for(int d=0;d<C;d++) a+=Q[n*C+d]*b2f(wk[c*C+d]); ws[2048+i]=a;}
}
__global__ void __launch_bounds__(256) mainK(const u16* f0,const u16* f1,const u16* wk,float* ws){
  extern __shared__ float sm[];
  float* feats=sm;
  float* Qk=sm+256*97;
  float* P=Qk+1536;
  float* red=P+16*256;
  int t=threadIdx.x; int px=blockIdx.x*256+t; int y=px/W,x=px%W; int p1=(y/2)*(W/2)+x/2;
  for(int i=t;i<NQ*C;i+=256) Qk[i]=ws[(MIMIC?0:2048)+i];
  float m=0;
  for(int c=0;c<C;c++){ float v=b2f(f0[(size_t)px*C+c])+b2f(f1[(size_t)p1*C+c]); if(MIMIC) v=rb(v); feats[t*97+c]=v; m+=v; }
  m/=C; float v=0; for(int c=0;c<C;c++){float d=feats[t*97+c]-m; v+=d*d;} v/=C;
  if(MIMIC){ m=rb(m); v=rb(v); float s=rb(sqrtf(rb(v+1e-5f))); for(int c=0;c<C;c++) feats[t*97+c]=rb(rb(feats[t*97+c]-m)/s);}
  else { float r=1.f/sqrtf(v+1e-5f); for(int c=0;c<C;c++) feats[t*97+c]=(feats[t*97+c]-m)*r; }
  __syncthreads();
  float s[NQ];
  #pragma unroll
  for(int n=0;n<NQ;n++) s[n]=0;
  if(MIMIC){
    for(int d=0;d<C;d++){ float k=0; for(int c=0;c<C;c++) k+=feats[t*97+c]*b2f(wk[c*C+d]); k=rb(k);
      #pragma unroll
      for(int n=0;n<NQ;n++) s[n]+=Qk[n*C+d]*k; }
    const float sc=0.10206207261596575f;
    #pragma unroll
    for(int n=0;n<NQ;n++) s[n]=rb(rb(s[n])*sc);
  } else {
    for(int c=0;c<C;c++){ float f=feats[t*97+c];
      #pragma unroll
      for(int n=0;n<NQ;n++) s[n]+=Qk[n*C+c]*f; }
    #pragma unroll
    for(int n=0;n<NQ;n++) s[n]*=0.10206207261596575f;
  }
  #pragma unroll
  for(int n=0;n<NQ;n++) P[n*256+t]=s[n];
  __syncthreads();
  if(t<NQ){ float mx=-INFINITY; for(int i=0;i<256;i++) mx=fmaxf(mx,P[t*256+i]); float l=0; for(int i=0;i<256;i++){ float e=__expf(P[t*256+i]-mx); P[t*256+i]=e; l+=e;} red[t*2]=mx; red[t*2+1]=l; }
  __syncthreads();
  float* part=ws+4096+(size_t)blockIdx.x*(1536+32);
  for(int i=t;i<NQ*C;i+=256){int n=i/C,c=i%C; float a=0; for(int j=0;j<256;j++) a+=P[n*256+j]*feats[j*97+c]; part[i]=a;}
  if(t<32) part[1536+t]=red[t];
}
__global__ void comb(const u16* q,const u16* wv,const u16* wo,const float* ws,__hip_bfloat16* out){
  __shared__ float A[C], ctx[C];
  int n=blockIdx.x,t=threadIdx.x;
  float M=-INFINITY; for(int b=0;b<NB;b++) M=fmaxf(M,ws[4096+(size_t)b*1568+1536+n*2]);
  float L=0,a=0; for(int b=0;b<NB;b++){ const float* p=ws+4096+(size_t)b*1568; float w=__expf(p[1536+n*2]-M); L+=w*p[1536+n*2+1]; a+=w*p[n*C+t]; }
  A[t]=a/L; __syncthreads();
  float c_=0; for(int c=0;c<C;c++) c_+=A[c]*b2f(wv[c*C+t]); ctx[t]=MIMIC?rb(c_):c_; __syncthreads();
  float o=0; for(int d=0;d<C;d++) o+=ctx[d]*b2f(wo[d*C+t]); if(MIMIC) o=rb(o);
  out[n*C+t]=__float2bfloat16(b2f(q[n*C+t])+o);
}
extern "C" void kernel_launch(void* const* d_in, const int* in_sizes, int n_in, void* d_out, int out_size, void* d_ws, size_t ws_size, hipStream_t stream) {
  const u16* q=(const u16*)d_in[0]; const u16* f0=(const u16*)d_in[1]; const u16* f1=(const u16*)d_in[2];
  const u16* wq=(const u16*)d_in[3]; const u16* wk=(const u16*)d_in[4]; const u16* wv=(const u16*)d_in[5]; const u16* wo=(const u16*)d_in[6];
  float* ws=(float*)d_ws;
  size_t lds=(256*97+1536+16*256+32)*4;
  hipFuncSetAttribute((const void*)mainK, hipFuncAttributeMaxDynamicSharedMemorySize, (int)lds);
  prep<<<1,256,0,stream>>>(q,wq,wk,ws);
  mainK<<<NB,256,lds,stream>>>(f0,f1,wk,ws);
  comb<<<NQ,C,0,stream>>>(q,wv,wo,ws,(__hip_bfloat16*)d_out);
}
```

```cpp
#include <hip/hip_runtime.h>
#include <hip/hip_bf16.h>
#pragma clang diagnostic ignored "-Wint-to-pointer-cast"

#define LAS __attribute__((address_space(3)))
#define GAS __attribute__((address_space(1)))
typedef unsigned u32;
typedef unsigned v4u __attribute__((ext_vector_type(4)));
typedef unsigned v2u __attribute__((ext_vector_type(2)));
typedef float f4 __attribute__((ext_vector_type(4)));
typedef float f2 __attribute__((ext_vector_type(2)));
typedef __bf16 bf8 __attribute__((ext_vector_type(8)));
typedef __bf16 bf4 __attribute__((ext_vector_type(4)));
typedef __bf16 bf2 __attribute__((ext_vector_type(2)));
typedef short s4 __attribute__((ext_vector_type(4)));

#define NT 1
#define NTS " nt"

constexpr int ZONE = 15360;
constexpr int MISC = 9 * ZONE;
constexpr int L_FLAG = MISC;
constexpr int L_SUM = MISC + 128;
constexpr int L_QF = MISC + 512;
constexpr int L_KF = L_QF + 3072;
constexpr int L_RED = L_KF + 3072;
constexpr int LDS_BYTES = L_RED + 1024;
constexpr u32 WS_REC = 4096;
constexpr u32 WS_STAT = WS_REC + 256 * 6144;
constexpr u32 WS_END = WS_STAT + 256 * 256;
constexpr float EPS96 = 9216.0f * 1e-5f;
constexpr float QSCALE = 14.135507f;
constexpr float THR = 40.0f;

#define STAMP(k)

__device__ __forceinline__ f4 mfma32(v4u a, v4u b, f4 c) {
  return __builtin_amdgcn_mfma_f32_16x16x32_bf16(__builtin_bit_cast(bf8, a), __builtin_bit_cast(bf8, b), c, 0, 0, 0);
}
__device__ __forceinline__ f4 mfma16(v2u a, v2u b, f4 c) {
  return __builtin_amdgcn_mfma_f32_16x16x16bf16_1k(__builtin_bit_cast(s4, a), __builtin_bit_cast(s4, b), c, 0, 0, 0);
}
__device__ __forceinline__ u32 pk(float lo, float hi) {
  f2 v = {lo, hi};
  return __builtin_bit_cast(u32, __builtin_convertvector(v, bf2));
}
__device__ __forceinline__ v4u pk8(f4 a, f4 b) { return v4u{pk(a[0], a[1]), pk(a[2], a[3]), pk(b[0], b[1]), pk(b[2], b[3])}; }
__device__ __forceinline__ float dot2(u32 a, u32 b, float c) {
  return __builtin_amdgcn_fdot2_f32_bf16(__builtin_bit_cast(bf2, a), __builtin_bit_cast(bf2, b), c, false);
}
__device__ __forceinline__ float dot8(v4u a, v4u b, float c) {
  c = dot2(a[0], b[0], c); c = dot2(a[1], b[1], c); c = dot2(a[2], b[2], c); return dot2(a[3], b[3], c);
}
__device__ __forceinline__ float blo(u32 x) { return __uint_as_float(x << 16); }
__device__ __forceinline__ float bhi(u32 x) { return __uint_as_float(x & 0xffff0000u); }
__device__ __forceinline__ float xg_sum(float x) {
  auto s = __builtin_amdgcn_permlane16_swap(__float_as_uint(x), __float_as_uint(x), false, false);
  x = __uint_as_float(s[0]) + __uint_as_float(s[1]);
  auto t = __builtin_amdgcn_permlane32_swap(__float_as_uint(x), __float_as_uint(x), false, false);
  return __uint_as_float(t[0]) + __uint_as_float(t[1]);
}
__device__ __forceinline__ float xg_max(float x) {
  auto s = __builtin_amdgcn_permlane16_swap(__float_as_uint(x), __float_as_uint(x), false, false);
  x = fmaxf(__uint_as_float(s[0]), __uint_as_float(s[1]));
  auto t = __builtin_amdgcn_permlane32_swap(__float_as_uint(x), __float_as_uint(x), false, false);
  return fmaxf(__uint_as_float(t[0]), __uint_as_float(t[1]));
}
template <int CTRL> __device__ __forceinline__ float dpp(float x) {
  return __builtin_bit_cast(float, __builtin_amdgcn_mov_dpp(__builtin_bit_cast(int, x), CTRL, 0xf, 0xf, true));
}
__device__ __forceinline__ v4u lds128(u32 a) { return *(const LAS v4u*)a; }
__device__ __forceinline__ v2u ldstr(u32 a) {
  return __builtin_bit_cast(v2u, __builtin_amdgcn_ds_read_tr16_b64_v4bf16((LAS bf4*)a));
}
__device__ __forceinline__ void set_flag(u32 a, u32 v) {
  asm volatile("" ::: "memory");
  __hip_atomic_store((LAS u32*)a, v, __ATOMIC_RELAXED, __HIP_MEMORY_SCOPE_WORKGROUP);
}
__device__ __forceinline__ void wait1(u32 a) {
  for (int sp = 0; sp < (1 << 20); sp++) {
    if (__builtin_amdgcn_readfirstlane(__hip_atomic_load((LAS u32*)a, __ATOMIC_RELAXED, __HIP_MEMORY_SCOPE_WORKGROUP)) != 0u) break;
    __builtin_amdgcn_s_sleep(1);
  }
  asm volatile("" ::: "memory");
}
__device__ __forceinline__ void wait3(u32 a) {
  for (int sp = 0; sp < (1 << 20); sp++) {
    asm volatile("" ::: "memory");
    v4u f = *(volatile const LAS v4u*)a;
    if (__builtin_amdgcn_readfirstlane(f[0] & f[1] & f[2])) break;
    __builtin_amdgcn_s_sleep(1);
  }
  asm volatile("" ::: "memory");
}
__device__ __forceinline__ void dma3(const void* base, u32 lds, u32 v0, u32 v1, u32 v2) {
  asm volatile("s_mov_b32 m0, %1\n\ts_nop 0\n\t"
               "global_load_lds_dwordx4 %2, %0" NTS "\n\t"
               "global_load_lds_dwordx4 %3, %0 offset:1024" NTS "\n\t"
               "global_load_lds_dwordx4 %4, %0 offset:2048" NTS
               :: "s"(base), "s"(lds), "v"(v0), "v"(v1), "v"(v2) : "memory");
}

__global__ void __launch_bounds__(768) dino_kernel(const char* __restrict__ q, const char* __restrict__ f0, const char* __restrict__ f1,
                                                   const char* __restrict__ wq, const char* __restrict__ wk, const char* __restrict__ wv,
                                                   const char* __restrict__ wo, unsigned short* __restrict__ out, char* __restrict__ ws) {
  extern __shared__ __attribute__((aligned(16))) char sm[];
  const u32 lb = (u32)(size_t)(LAS char*)sm;
  const int wave = __builtin_amdgcn_readfirstlane(threadIdx.x >> 6), lane = threadIdx.x & 63;
  const int g = lane >> 4, p = lane & 15;
  if (threadIdx.x < 32) *(LAS u32*)(lb + L_FLAG + 4 * threadIdx.x) = 0u;
  asm volatile("s_waitcnt lgkmcnt(0)" ::: "memory");
  __builtin_amdgcn_s_barrier();
  asm volatile("" ::: "memory");
  const __amdgpu_buffer_rsrc_t wr = __builtin_amdgcn_make_buffer_rsrc(ws, 0, WS_END, 0x00020000);

  if (wave < 3) {
    const int h = wave;
    const int rowp = 8 * (p >> 2) + (p & 3);
    const u32 go = rowp * 192 + g * 16;
    v4u qf[3], wqf[6], wkf[6];
#pragma unroll
    for (int s = 0; s < 3; s++) qf[s] = *(const v4u*)(q + p * 192 + s * 64 + g * 16);
#pragma unroll
    for (int i = 0; i < 6; i++) wqf[i] = *(const v4u*)(wq + go + (32 * (i >> 1) + 4 * (i & 1)) * 192 + h * 64);
#pragma unroll
    for (int i = 0; i < 6; i++) wkf[i] = *(const v4u*)(wk + go + (32 * h + 4 * (i & 1)) * 192 + (i >> 1) * 64);
    const u32 ep = __builtin_amdgcn_raw_buffer_load_b32(wr, 0, 0, 16);
    v4u sel[2];
    {
      const bool on = (g == (p >> 2));
      const u32 one = (p & 1) ? 0x3f800000u : 0x00003f80u;
      const u32 a = (on && !(p & 2)) ? one : 0u, b = (on && (p & 2)) ? one : 0u;
      sel[0] = v4u{a, b, 0u, 0u}; sel[1] = v4u{0u, 0u, a, b};
    }
    v4u qn[3];
    {
      float x[24]; float s1 = 0.f;
#pragma unroll
      for (int s = 0; s < 3; s++)
#pragma unroll
        for (int j = 0; j < 4; j++) { x[8 * s + 2 * j] = blo(qf[s][j]); x[8 * s + 2 * j + 1] = bhi(qf[s][j]); }
#pragma unroll
      for (int j = 0; j < 24; j++) s1 += x[j];
      const float mean = xg_sum(s1) * (1.0f / 96.0f);
      float s2 = 0.f;
#pragma unroll
      for (int j = 0; j < 24; j++) { x[j] -= mean; s2 = fmaf(x[j], x[j], s2); }
      const float r = __builtin_amdgcn_rsqf(xg_sum(s2) * (1.0f / 96.0f) + 1e-5f);
#pragma unroll
      for (int s = 0; s < 3; s++)
#pragma unroll
        for (int j = 0; j < 4; j++) qn[s][j] = pk(x[8 * s + 2 * j] * r, x[8 * s + 2 * j + 1] * r);
    }
    const f4 z4 = {0.f, 0.f, 0.f, 0.f};
    STAMP(1);
    {
      f4 Q[2] = {z4, z4};
#pragma unroll
      for (int s = 0; s < 3; s++)
#pragma unroll
        for (int hh = 0; hh < 2; hh++) {
          const v4u a = pk8(mfma32(wqf[2 * s], sel[hh], z4), mfma32(wqf[2 * s + 1], sel[hh], z4));
          Q[hh] = mfma32(a, qn[s], Q[hh]);
        }
      *(LAS v4u*)(lb + L_QF + h * 1024 + lane * 16) = pk8(Q[0], Q[1]);
      set_flag(lb + L_FLAG + 4 * h, 1u);
    }
    wait3(lb + L_FLAG);
    {
      f4 K[2] = {z4, z4};
#pragma unroll
      for (int u = 0; u < 3; u++) {
        const v4u b = lds128(lb + L_QF + u * 1024 + lane * 16);
        K[0] = mfma32(wkf[2 * u], b, K[0]); K[1] = mfma32(wkf[2 * u + 1], b, K[1]);
      }
      const float rs = xg_sum((K[0][0] + K[0][1]) + (K[0][2] + K[0][3]) + (K[1][0] + K[1][1]) + (K[1][2] + K[1][3]));
      *(LAS float*)(lb + L_SUM + h * 64 + p * 4) = rs;
      set_flag(lb + L_FLAG + 16 + 4 * h, 1u);
      wait3(lb + L_FLAG + 16);
      const float mean = (*(const LAS float*)(lb + L_SUM + p * 4) + *(const LAS float*)(lb + L_SUM + 64 + p * 4) +
                          *(const LAS float*)(lb + L_SUM + 128 + p * 4)) * (1.0f / 96.0f);
      K[0] = (K[0] - mean) * QSCALE; K[1] = (K[1] - mean) * QSCALE;
      *(LAS v4u*)(lb + L_KF + h * 1024 + lane * 16) = pk8(K[0], K[1]);
      if (h == 0) { *(LAS u32*)(lb + L_FLAG + 96) = (ep + 1u) & 0xfffu; *(LAS u32*)(lb + L_FLAG + 100) = ep + 1u; }
      set_flag(lb + L_FLAG + 32 + 4 * h, 1u);
    }
    STAMP(2);
    v4u wf[2][3];
    {
      v4u wof[6], wvf[18];
#pragma unroll
      for (int i = 0; i < 6; i++) wof[i] = *(const v4u*)(wo + go + (32 * (i >> 1) + 4 * (i & 1)) * 192 + h * 64);
#pragma unroll
      for (int i = 0; i < 18; i++) wvf[i] = *(const v4u*)(wv + (16 * (i / 3) + p) * 192 + (i % 3) * 64 + g * 16);
      v4u bo[2][3];
#pragma unroll
      for (int s = 0; s < 3; s++)
#pragma unroll
        for (int hh = 0; hh < 2; hh++) bo[hh][s] = pk8(mfma32(wof[2 * s], sel[hh], z4), mfma32(wof[2 * s + 1], sel[hh], z4));
#pragma unroll
      for (int hh = 0; hh < 2; hh++) {
        f4 D[6]; float cs = 0.f;
#pragma unroll
        for (int t = 0; t < 6; t++) {
          D[t] = z4;
#pragma unroll
          for (int s = 0; s < 3; s++) D[t] = mfma32(wvf[3 * t + s], bo[hh][s], D[t]);
          cs += (D[t][0] + D[t][1]) + (D[t][2] + D[t][3]);
        }
        const float mean = xg_sum(cs) * (1.0f / 96.0f);
#pragma unroll
        for (int u = 0; u < 3; u++) wf[hh][u] = pk8((D[2 * u] - mean) * 96.0f, (D[2 * u + 1] - mean) * 96.0f);
      }
    }
    STAMP(3);
    float M = -INFINITY, L = 0.f; f4 O[2] = {z4, z4};
#pragma unroll 1
    for (int w = 0; w < 9; w++) {
      wait1(lb + L_FLAG + 48 + 4 * w);
      const u32 zb = lb + w * ZONE;
      const v4u b0 = lds128(zb + lane * 16), b1 = lds128(zb + 1024 + lane * 16), b2 = lds128(zb + 2048 + lane * 16);
      const f2 ml = *(const LAS f2*)(zb + 3072 + p * 8);
      f4 D0 = mfma32(wf[0][0], b0, z4), D1 = mfma32(wf[1][0], b0, z4);
      D0 = mfma32(wf[0][1], b1, D0); D1 = mfma32(wf[1][1], b1, D1);
      D0 = mfma32(wf[0][2], b2, D0); D1 = mfma32(wf[1][2], b2, D1);
      const float Mn = fmaxf(M, ml[0]);
      const float a = __builtin_amdgcn_exp2f(M - Mn), b = __builtin_amdgcn_exp2f(ml[0] - Mn);
      L = L * a + ml[1] * b; M = Mn;
      O[0] = O[0] * a + D0 * b; O[1] = O[1] * a + D1 * b;
    }
    STAMP(4);
    const u32 T = *(const LAS u32*)(lb + L_FLAG + 96);
#pragma unroll
    for (int hh = 0; hh < 2; hh++) {
      v4u o;
#pragma unroll
      for (int r = 0; r < 4; r++) o[r] = (__float_as_uint(O[hh][r]) & 0xfffff000u) | T;
      __builtin_amdgcn_raw_buffer_store_b128(o, wr, WS_REC + blockIdx.x * 6144 + p * 384 + (2 * h + hh) * 64 + g * 16, 0, 16);
    }
    if (h == 0 && g == 0) {
      const u32 mu = __float_as_uint(M), lu = __float_as_uint(L);
      const v4u o = {(mu & 0xfffff000u) | T, (mu << 20) | T, (lu & 0xfffff000u) | T, (lu << 20) | T};
      __builtin_amdgcn_raw_buffer_store_b128(o, wr, WS_STAT + blockIdx.x * 256 + p * 16, 0, 16);
    }
    STAMP(5);
    __builtin_amdgcn_s_barrier();
  } else {
    const int bw = wave - 3;
    const u32 z = lb + bw * ZONE;
    {
      const int st = blockIdx.x * 9 + bw, i = st / 12, j = st - i * 12;
      const char* a0 = f0 + (size_t)(i * 768 + 32 * j) * 192;
      const char* b = f1 + (size_t)(i * 192 + 16 * j) * 192;
      const u32 v0 = 16 * (lane ^ ((lane >= 48) ? 2 : 0));
      const u32 v1 = 16 * (lane ^ ((lane >= 32) ? 4 : 2));
      const u32 v2 = 16 * (lane ^ ((lane >= 16) ? 6 : 4));
      dma3(b, z, v0, v1, v2);
      dma3(a0, z + 3072, v0, v1, v2); dma3(a0 + 3072, z + 6144, v0, v1, v2);
      dma3(a0 + 384 * 192, z + 9216, v0, v1, v2); dma3(a0 + 384 * 192 + 3072, z + 12288, v0, v1, v2);
    }
    STAMP(1);
    const f4 z4 = {0.f, 0.f, 0.f, 0.f};
    const v4u ones = {0x3f803f80u, 0x3f803f80u, 0x3f803f80u, 0x3f803f80u};
    wait3(lb + L_FLAG + 32);
    v4u qk[3];
#pragma unroll
    for (int s = 0; s < 3; s++) qk[s] = lds128(lb + L_KF + s * 1024 + lane * 16);
    STAMP(2);
    asm volatile("s_waitcnt vmcnt(12)" ::: "memory");
    const int pt = 4 * g + (p >> 2);
    const u32 tb = ((p >> 1) & 1), th = 8 * (p & 1);
    v4u a1[3]; v2u t1[6];
#pragma unroll
    for (int s = 0; s < 3; s++) a1[s] = lds128(z + 16 * ((12 * p + 4 * s + g) ^ ((p >> 2) << 1)));
#pragma unroll
    for (int t = 0; t < 6; t++) t1[t] = ldstr(z + 16 * ((12 * pt + 2 * t + tb) ^ (g << 1)) + th);
    f4 S1 = z4, X1 = z4; float q11 = 0.f;
#pragma unroll
    for (int s = 0; s < 3; s++) { S1 = mfma32(a1[s], qk[s], S1); X1 = mfma32(ones, a1[s], X1); q11 = dot8(a1[s], a1[s], q11); }
    const u32 rb0 = 384 * p + 16 * (g ^ (((p >> 1) & 3) << 1));
    const u32 tb0 = 384 * pt + 16 * ((((pt >> 1) & 3) << 1) | tb) + th;
    const float oh0 = (g == 0) ? 1.f : 0.f, oh1 = (g == 2) ? 1.f : 0.f;
    float m = 0.f, l = 0.f; f4 W1 = z4; f4 acc[6];
#pragma unroll
    for (int t = 0; t < 6; t++) acc[t] = z4;
#pragma unroll 1
    for (int it = 0; it < 2; it++) {
      if (it == 0) asm volatile("s_waitcnt vmcnt(6)" ::: "memory"); else asm volatile("s_waitcnt vmcnt(0)" ::: "memory");
      const u32 zb = z + 3072 + it * 6144;
      const u32 r0 = zb + rb0, r1 = zb + (rb0 ^ 64);
      v4u a[2][3];
      a[0][0] = lds128(r0); a[0][1] = lds128(r1); a[0][2] = lds128(r0 + 128);
      a[1][0] = lds128(r1 + 128); a[1][1] = lds128(r0 + 256); a[1][2] = lds128(r1 + 256);
      f4 S[2], X[2]; float sq[2];
#pragma unroll
      for (int dx = 0; dx < 2; dx++) {
        S[dx] = S1; X[dx] = X1; float d00 = q11, d01 = 0.f;
#pragma unroll
        for (int s = 0; s < 3; s++) {
          S[dx] = mfma32(a[dx][s], qk[s], S[dx]); X[dx] = mfma32(ones, a[dx][s], X[dx]);
          d00 = dot8(a[dx][s], a[dx][s], d00); d01 = dot8(a[dx][s], a1[s], d01);
        }
        sq[dx] = fmaf(2.0f, d01, d00);
      }
      float rr;
      {
        auto s = __builtin_amdgcn_permlane32_swap(__float_as_uint(sq[0]), __float_as_uint(sq[1]), false, false);
        float y = __uint_as_float(s[0]) + __uint_as_float(s[1]);
        auto t = __builtin_amdgcn_permlane16_swap(__float_as_uint(y), __float_as_uint(y), false, false);
        y = __uint_as_float(t[0]) + __uint_as_float(t[1]);
        const float sx = (lane < 32) ? X[0][0] : X[1][0];
        const float v = fmaxf(fmaf(-sx, sx, fmaf(y, 96.0f, EPS96)), EPS96);
        rr = __builtin_amdgcn_rsqf(v);
      }
      f4 R[2];
      R[0] = __builtin_amdgcn_mfma_f32_16x16x4f32(rr, oh0, z4, 0, 0, 0);
      R[1] = __builtin_amdgcn_mfma_f32_16x16x4f32(rr, oh1, z4, 0, 0, 0);
      v4u at[6];
#pragma unroll
      for (int t = 0; t < 6; t++) {
        const int c0 = 2 * t, c1 = 12 + 2 * t;
        const v2u x0 = ldstr(zb + (tb0 ^ (16 * (c0 & 6))) + 16 * (c0 & ~7));
        const v2u x1 = ldstr(zb + (tb0 ^ (16 * (c1 & 6))) + 16 * (c1 & ~7));
        at[t] = v4u{x0[0], x0[1], x1[0], x1[1]};
      }
      f4 u[2];
#pragma unroll
      for (int dx = 0; dx < 2; dx++)
#pragma unroll
        for (int r = 0; r < 4; r++) u[dx][r] = fmaf(S[dx][r], R[dx][r], -m);
      const float lm = fmaxf(fmaxf(fmaxf(u[0][0], u[0][1]), fmaxf(u[0][2], u[0][3])), fmaxf(fmaxf(u[1][0], u[1][1]), fmaxf(u[1][2], u[1][3])));
      if (it == 0) {
        const float d = xg_max(lm);
        m = d; u[0] -= d; u[1] -= d;
      } else if (__builtin_amdgcn_ballot_w64(lm > THR) != 0ull) {
        const float d = fmaxf(xg_max(lm), 0.f);
        const float al = __builtin_amdgcn_exp2f(-d);
        m += d; u[0] -= d; u[1] -= d; l *= al; W1 *= al;
#pragma unroll
        for (int t = 0; t < 6; t++) acc[t] *= al;
      }
      f4 P[2];
#pragma unroll
      for (int dx = 0; dx < 2; dx++)
#pragma unroll
        for (int r = 0; r < 4; r++) { const float e = __builtin_amdgcn_exp2f(u[dx][r]); l += e; P[dx][r] = e * R[dx][r]; }
      W1 += P[0] + P[1];
      const v4u pb = pk8(P[0], P[1]);
#pragma unroll
      for (int t = 0; t < 6; t++) acc[t] = mfma32(at[t], pb, acc[t]);
    }
    {
      const v2u wb = {pk(W1[0], W1[1]), pk(W1[2], W1[3])};
#pragma unroll
      for (int t = 0; t < 6; t++) acc[t] = mfma16(t1[t], wb, acc[t]);
    }
    l = xg_sum(l);
#pragma unroll
    for (int u = 0; u < 3; u++) *(LAS v4u*)(z + u * 1024 + lane * 16) = pk8(acc[2 * u], acc[2 * u + 1]);
    *(LAS f2*)(z + 3072 + p * 8) = f2{m, l};
    set_flag(lb + L_FLAG + 48 + 4 * bw, 1u);
    STAMP(4);
    __builtin_amdgcn_s_barrier();
    asm volatile("" ::: "memory");
    STAMP(5);
    if (blockIdx.x < 96 && bw < 8) {
      const int n = 2 * (blockIdx.x & 7) + ((blockIdx.x >> 3) & 1), t = blockIdx.x >> 4;
      const u32 T = *(const LAS u32*)(lb + L_FLAG + 96);
      const int pr = 32 * bw + (lane >> 2);
      const u32 od = WS_REC + pr * 6144 + n * 384 + t * 64 + (lane & 3) * 16, os = WS_STAT + pr * 256 + n * 16;
      const int e = 32 * (t >> 1) + 8 * ((lane >> 2) & 3) + 4 * (t & 1) + (lane & 3);
      const unsigned short qv = ((const unsigned short*)q)[n * 96 + e];
      v4u d0, d1, s0, s1;
      int spins = 0;
      for (;;) {
        asm volatile("" ::: "memory");
        d0 = __builtin_amdgcn_raw_buffer_load_b128(wr, od, 0, 16);
        s0 = __builtin_amdgcn_raw_buffer_load_b128(wr, os, 0, 16);
        d1 = __builtin_amdgcn_raw_buffer_load_b128(wr, od + 16 * 6144, 0, 16);
        s1 = __builtin_amdgcn_raw_buffer_load_b128(wr, os + 16 * 256, 0, 16);
        const v4u x = (d0 ^ T) | (d1 ^ T) | (s0 ^ T) | (s1 ^ T);
        if ((((x[0] | x[1]) | (x[2] | x[3])) & 0xfffu) == 0u) break;
        if (++spins > (1 << 16)) break;
      }
      STAMP(6);
      const float m0 = __uint_as_float((s0[0] & 0xfffff000u) | (s0[1] >> 20)), l0 = __uint_as_float((s0[2] & 0xfffff000u) | (s0[3] >> 20));
      const float m1 = __uint_as_float((s1[0] & 0xfffff000u) | (s1[1] >> 20)), l1 = __uint_as_float((s1[2] & 0xfffff000u) | (s1[3] >> 20));
      float M = fmaxf(m0, m1);
      M = fmaxf(M, dpp<0x124>(M)); M = fmaxf(M, dpp<0x128>(M)); M = xg_max(M);
      const float w0 = __builtin_amdgcn_exp2f(m0 - M), w1 = __builtin_amdgcn_exp2f(m1 - M);
      float v[5];
#pragma unroll
      for (int r = 0; r < 4; r++) v[r] = __uint_as_float(d0[r]) * w0 + __uint_as_float(d1[r]) * w1;
      v[4] = l0 * w0 + l1 * w1;
#pragma unroll
      for (int r = 0; r < 5; r++) { v[r] += dpp<0x124>(v[r]); v[r] += dpp<0x128>(v[r]); v[r] = xg_sum(v[r]); }
      const u32 rbase = lb + L_RED + bw * 80;
      if (lane < 4) { *(LAS f4*)(rbase + lane * 16) = f4{v[0], v[1], v[2], v[3]}; *(LAS f2*)(rbase + 64) = f2{M, v[4]}; }
      asm volatile("s_waitcnt lgkmcnt(0)" ::: "memory");
      __builtin_amdgcn_s_barrier();
      asm volatile("" ::: "memory");
      if (bw == 0 && lane < 16) {
        float o[8]; f2 ml[8];
#pragma unroll
        for (int w = 0; w < 8; w++) { o[w] = *(const LAS float*)(lb + L_RED + w * 80 + lane * 4); ml[w] = *(const LAS f2*)(lb + L_RED + w * 80 + 64); }
        float MM = ml[0][0];
#pragma unroll
        for (int w = 1; w < 8; w++) MM = fmaxf(MM, ml[w][0]);
        float LL = 0.f, OO = 0.f;
#pragma unroll
        for (int w = 0; w < 8; w++) { const float ww = __builtin_amdgcn_exp2f(ml[w][0] - MM); LL = fmaf(ww, ml[w][1], LL); OO = fmaf(ww, o[w], OO); }
        const float res = __uint_as_float((u32)qv << 16) + OO * __builtin_amdgcn_rcpf(LL);
        out[n * 96 + e] = (unsigned short)(pk(res, res) & 0xffffu);
        if (blockIdx.x == 0 && lane == 0) *(u32*)ws = *(const LAS u32*)(lb + L_FLAG + 100);
      }
      STAMP(7);
    }
  }
}

extern "C" void kernel_launch(void* const* d_in, const int* in_sizes, int n_in, void* d_out, int out_size, void* d_ws, size_t ws_size,
                              hipStream_t stream) {
  (void)hipFuncSetAttribute((const void*)dino_kernel, hipFuncAttributeMaxDynamicSharedMemorySize, LDS_BYTES);
  dino_kernel<<<256, 768, LDS_BYTES, stream>>>((const char*)d_in[0], (const char*)d_in[1], (const char*)d_in[2], (const char*)d_in[3],
                                               (const char*)d_in[4], (const char*)d_in[5], (const char*)d_in[6], (unsigned short*)d_out,
                                               (char*)d_ws);
}
```

```cpp
#define PROBE 0
#define NT 1
#define FENCE 1
#include <hip/hip_runtime.h>
#include <hip/hip_bf16.h>
#pragma clang diagnostic ignored "-Wint-to-pointer-cast"

#define LAS __attribute__((address_space(3)))
#define GAS __attribute__((address_space(1)))
typedef unsigned u32;
typedef unsigned v4u __attribute__((ext_vector_type(4)));
typedef unsigned v2u __attribute__((ext_vector_type(2)));
typedef float f4 __attribute__((ext_vector_type(4)));
typedef float f2 __attribute__((ext_vector_type(2)));
typedef __bf16 bf8 __attribute__((ext_vector_type(8)));
typedef __bf16 bf4 __attribute__((ext_vector_type(4)));
typedef __bf16 bf2 __attribute__((ext_vector_type(2)));
typedef short s4 __attribute__((ext_vector_type(4)));

#define NTS " nt"

constexpr int ZONE = 15360;
constexpr int MISC = 9 * ZONE;
constexpr int L_FLAG = MISC;
constexpr int L_SUM = MISC + 128;
constexpr int L_QF = MISC + 512;
constexpr int L_KF = L_QF + 3072;
constexpr int L_RED = L_KF + 3072;
constexpr int LDS_BYTES = L_RED + 1024;
constexpr u32 WS_REC = 4096;
constexpr u32 WS_STAT = WS_REC + 256 * 6144;
constexpr u32 WS_END = WS_STAT + 256 * 256;
constexpr float EPS96 = 9216.0f * 1e-5f;
constexpr float QSCALE = 14.135507f;
constexpr float THR = 40.0f;

#define STAMP(k)

__device__ __forceinline__ f4 mfma32(v4u a, v4u b, f4 c) {
  return __builtin_amdgcn_mfma_f32_16x16x32_bf16(__builtin_bit_cast(bf8, a), __builtin_bit_cast(bf8, b), c, 0, 0, 0);
}
__device__ __forceinline__ f4 mfma16(v2u a, v2u b, f4 c) {
  return __builtin_amdgcn_mfma_f32_16x16x16bf16_1k(__builtin_bit_cast(s4, a), __builtin_bit_cast(s4, b), c, 0, 0, 0);
}
__device__ __forceinline__ u32 pk(float lo, float hi) {
  f2 v = {lo, hi};
  return __builtin_bit_cast(u32, __builtin_convertvector(v, bf2));
}
__device__ __forceinline__ v4u pk8(f4 a, f4 b) { return v4u{pk(a[0], a[1]), pk(a[2], a[3]), pk(b[0], b[1]), pk(b[2], b[3])}; }
__device__ __forceinline__ float dot2(u32 a, u32 b, float c) {
  return __builtin_amdgcn_fdot2_f32_bf16(__builtin_bit_cast(bf2, a), __builtin_bit_cast(bf2, b), c, false);
}
__device__ __forceinline__ float dot8(v4u a, v4u b, float c) {
  c = dot2(a[0], b[0], c); c = dot2(a[1], b[1], c); c = dot2(a[2], b[2], c); return dot2(a[3], b[3], c);
}
__device__ __forceinline__ float blo(u32 x) { return __uint_as_float(x << 16); }
__device__ __forceinline__ float bhi(u32 x) { return __uint_as_float(x & 0xffff0000u); }
__device__ __forceinline__ float xg_sum(float x) {
  auto s = __builtin_amdgcn_permlane16_swap(__float_as_uint(x), __float_as_uint(x), false, false);
  x = __uint_as_float(s[0]) + __uint_as_float(s[1]);
  auto t = __builtin_amdgcn_permlane32_swap(__float_as_uint(x), __float_as_uint(x), false, false);
  return __uint_as_float(t[0]) + __uint_as_float(t[1]);
}
__device__ __forceinline__ float xg_max(float x) {
  auto s = __builtin_amdgcn_permlane16_swap(__float_as_uint(x), __float_as_uint(x), false, false);
  x = fmaxf(__uint_as_float(s[0]), __uint_as_float(s[1]));
  auto t = __builtin_amdgcn_permlane32_swap(__float_as_uint(x), __float_as_uint(x), false, false);
  return fmaxf(__uint_as_float(t[0]), __uint_as_float(t[1]));
}
template <int CTRL> __device__ __forceinline__ float dpp(float x) {
  return __builtin_bit_cast(float, __builtin_amdgcn_mov_dpp(__builtin_bit_cast(int, x), CTRL, 0xf, 0xf, true));
}
__device__ __forceinline__ v4u lds128(u32 a) { return *(const LAS v4u*)a; }
__device__ __forceinline__ v2u ldstr(u32 a) {
  return __builtin_bit_cast(v2u, __builtin_amdgcn_ds_read_tr16_b64_v4bf16((LAS bf4*)a));
}
__device__ __forceinline__ void set_flag(u32 a, u32 v) {
  asm volatile("" ::: "memory");
  __hip_atomic_store((LAS u32*)a, v, __ATOMIC_RELAXED, __HIP_MEMORY_SCOPE_WORKGROUP);
}
__device__ __forceinline__ void wait1(u32 a) {
  for (int sp = 0; sp < (1 << 20); sp++) {
    if (__builtin_amdgcn_readfirstlane(__hip_atomic_load((LAS u32*)a, __ATOMIC_RELAXED, __HIP_MEMORY_SCOPE_WORKGROUP)) != 0u) break;
    __builtin_amdgcn_s_sleep(1);
  }
  asm volatile("" ::: "memory");
}
__device__ __forceinline__ void wait3(u32 a) {
  for (int sp = 0; sp < (1 << 20); sp++) {
    asm volatile("" ::: "memory");
    v4u f = *(volatile const LAS v4u*)a;
    if (__builtin_amdgcn_readfirstlane(f[0] & f[1] & f[2])) break;
    __builtin_amdgcn_s_sleep(1);
  }
  asm volatile("" ::: "memory");
}
__device__ __forceinline__ void dma3(const void* base, u32 lds, u32 v0, u32 v1, u32 v2) {
  asm volatile("s_mov_b32 m0, %1\n\ts_nop 0\n\t"
               "global_load_lds_dwordx4 %2, %0" NTS "\n\t"
               "global_load_lds_dwordx4 %3, %0 offset:1024" NTS "\n\t"
               "global_load_lds_dwordx4 %4, %0 offset:2048" NTS
               :: "s"(base), "s"(lds), "v"(v0), "v"(v1), "v"(v2) : "memory");
}

__global__ void __launch_bounds__(768) dino_kernel(const char* __restrict__ q, const char* __restrict__ f0, const char* __restrict__ f1,
                                                   const char* __restrict__ wq, const char* __restrict__ wk, const char* __restrict__ wv,
                                                   const char* __restrict__ wo, unsigned short* __restrict__ out, char* __restrict__ ws) {
  extern __shared__ __attribute__((aligned(16))) char sm[];
  const unsigned long long pt0 = __builtin_amdgcn_s_memrealtime(); const unsigned long long pc0 = __builtin_amdgcn_s_memtime();
  const u32 lb = (u32)(size_t)(LAS char*)sm;
#define PSET() do { unsigned long long t_; asm volatile("s_memrealtime %0\n\ts_waitcnt lgkmcnt(0)" : "=s"(t_) :: "memory"); *(volatile LAS u32*)(lb + L_FLAG + 108) = (u32)(t_ - pt0); } while (0)
  const int wave = __builtin_amdgcn_readfirstlane(threadIdx.x >> 6), lane = threadIdx.x & 63;
  const int g = lane >> 4, p = lane & 15;
  if (threadIdx.x < 32) *(LAS u32*)(lb + L_FLAG + 4 * threadIdx.x) = 0u;
  asm volatile("s_waitcnt lgkmcnt(0)" ::: "memory");
  __builtin_amdgcn_s_barrier();
  asm volatile("" ::: "memory");
  const __amdgpu_buffer_rsrc_t wr = __builtin_amdgcn_make_buffer_rsrc(ws, 0, WS_END, 0x00020000);

  if (wave < 3) {
    const int h = wave;
    if (FENCE && h == 0) { __builtin_amdgcn_fence(__ATOMIC_RELEASE, "agent"); asm volatile("s_waitcnt vmcnt(0)" ::: "memory"); if (PROBE == 8) PSET(); }
    const int rowp = 8 * (p >> 2) + (p & 3);
    const u32 go = rowp * 192 + g * 16;
    v4u qf[3], wqf[6], wkf[6];
#pragma unroll
    for (int s = 0; s < 3; s++) qf[s] = *(const v4u*)(q + p * 192 + s * 64 + g * 16);
#pragma unroll
    for (int i = 0; i < 6; i++) wqf[i] = *(const v4u*)(wq + go + (32 * (i >> 1) + 4 * (i & 1)) * 192 + h * 64);
#pragma unroll
    for (int i = 0; i < 6; i++) wkf[i] = *(const v4u*)(wk + go + (32 * h + 4 * (i & 1)) * 192 + (i >> 1) * 64);
    const u32 ep = __builtin_amdgcn_raw_buffer_load_b32(wr, 0, 0, 16);
    v4u sel[2];
    {
      const bool on = (g == (p >> 2));
      const u32 one = (p & 1) ? 0x3f800000u : 0x00003f80u;
      const u32 a = (on && !(p & 2)) ? one : 0u, b = (on && (p & 2)) ? one : 0u;
      sel[0] = v4u{a, b, 0u, 0u}; sel[1] = v4u{0u, 0u, a, b};
    }
    v4u qn[3];
    {
      float x[24]; float s1 = 0.f;
#pragma unroll
      for (int s = 0; s < 3; s++)
#pragma unroll
        for (int j = 0; j < 4; j++) { x[8 * s + 2 * j] = blo(qf[s][j]); x[8 * s + 2 * j + 1] = bhi(qf[s][j]); }
#pragma unroll
      for (int j = 0; j < 24; j++) s1 += x[j];
      const float mean = xg_sum(s1) * (1.0f / 96.0f);
      float s2 = 0.f;
#pragma unroll
      for (int j = 0; j < 24; j++) { x[j] -= mean; s2 = fmaf(x[j], x[j], s2); }
      const float r = __builtin_amdgcn_rsqf(xg_sum(s2) * (1.0f / 96.0f) + 1e-5f);
#pragma unroll
      for (int s = 0; s < 3; s++)
#pragma unroll
        for (int j = 0; j < 4; j++) qn[s][j] = pk(x[8 * s + 2 * j] * r, x[8 * s + 2 * j + 1] * r);
    }
    const f4 z4 = {0.f, 0.f, 0.f, 0.f};
    STAMP(1);
    {
      f4 Q[2] = {z4, z4};
#pragma unroll
      for (int s = 0; s < 3; s++)
#pragma unroll
        for (int hh = 0; hh < 2; hh++) {
          const v4u a = pk8(mfma32(wqf[2 * s], sel[hh], z4), mfma32(wqf[2 * s + 1], sel[hh], z4));
          Q[hh] = mfma32(a, qn[s], Q[hh]);
        }
      *(LAS v4u*)(lb + L_QF + h * 1024 + lane * 16) = pk8(Q[0], Q[1]);
      set_flag(lb + L_FLAG + 4 * h, 1u);
    }
    wait3(lb + L_FLAG);
    {
      f4 K[2] = {z4, z4};
#pragma unroll
      for (int u = 0; u < 3; u++) {
        const v4u b = lds128(lb + L_QF + u * 1024 + lane * 16);
        K[0] = mfma32(wkf[2 * u], b, K[0]); K[1] = mfma32(wkf[2 * u + 1], b, K[1]);
      }
      const float rs = xg_sum((K[0][0] + K[0][1]) + (K[0][2] + K[0][3]) + (K[1][0] + K[1][1]) + (K[1][2] + K[1][3]));
      *(LAS float*)(lb + L_SUM + h * 64 + p * 4) = rs;
      set_flag(lb + L_FLAG + 16 + 4 * h, 1u);
      wait3(lb + L_FLAG + 16);
      const float mean = (*(const LAS float*)(lb + L_SUM + p * 4) + *(const LAS float*)(lb + L_SUM + 64 + p * 4) +
                          *(const LAS float*)(lb + L_SUM + 128 + p * 4)) * (1.0f / 96.0f);
      K[0] = (K[0] - mean) * QSCALE; K[1] = (K[1] - mean) * QSCALE;
      *(LAS v4u*)(lb + L_KF + h * 1024 + lane * 16) = pk8(K[0], K[1]);
      if (h == 0) { *(LAS u32*)(lb + L_FLAG + 96) = (ep + 1u) & 0xfffu; *(LAS u32*)(lb + L_FLAG + 100) = ep + 1u; }
      set_flag(lb + L_FLAG + 32 + 4 * h, 1u);
    }
    STAMP(2);
    v4u wf[2][3];
    {
      v4u wof[6], wvf[18];
#pragma unroll
      for (int i = 0; i < 6; i++) wof[i] = *(const v4u*)(wo + go + (32 * (i >> 1) + 4 * (i & 1)) * 192 + h * 64);
#pragma unroll
      for (int i = 0; i < 18; i++) wvf[i] = *(const v4u*)(wv + (16 * (i / 3) + p) * 192 + (i % 3) * 64 + g * 16);
      v4u bo[2][3];
#pragma unroll
      for (int s = 0; s < 3; s++)
#pragma unroll
        for (int hh = 0; hh < 2; hh++) bo[hh][s] = pk8(mfma32(wof[2 * s], sel[hh], z4), mfma32(wof[2 * s + 1], sel[hh], z4));
#pragma unroll
      for (int hh = 0; hh < 2; hh++) {
        f4 D[6]; float cs = 0.f;
#pragma unroll
        for (int t = 0; t < 6; t++) {
          D[t] = z4;
#pragma unroll
          for (int s = 0; s < 3; s++) D[t] = mfma32(wvf[3 * t + s], bo[hh][s], D[t]);
          cs += (D[t][0] + D[t][1]) + (D[t][2] + D[t][3]);
        }
        const float mean = xg_sum(cs) * (1.0f / 96.0f);
#pragma unroll
        for (int u = 0; u < 3; u++) wf[hh][u] = pk8((D[2 * u] - mean) * 96.0f, (D[2 * u + 1] - mean) * 96.0f);
      }
    }
    STAMP(3);
    float M = -INFINITY, L = 0.f; f4 O[2] = {z4, z4};
#pragma unroll 1
    for (int w = 0; w < 9; w++) {
      wait1(lb + L_FLAG + 48 + 4 * w);
      const u32 zb = lb + w * ZONE;
      const v4u b0 = lds128(zb + lane * 16), b1 = lds128(zb + 1024 + lane * 16), b2 = lds128(zb + 2048 + lane * 16);
      const f2 ml = *(const LAS f2*)(zb + 3072 + p * 8);
      f4 D0 = mfma32(wf[0][0], b0, z4), D1 = mfma32(wf[1][0], b0, z4);
      D0 = mfma32(wf[0][1], b1, D0); D1 = mfma32(wf[1][1], b1, D1);
      D0 = mfma32(wf[0][2], b2, D0); D1 = mfma32(wf[1][2], b2, D1);
      const float Mn = fmaxf(M, ml[0]);
      const float a = __builtin_amdgcn_exp2f(M - Mn), b = __builtin_amdgcn_exp2f(ml[0] - Mn);
      L = L * a + ml[1] * b; M = Mn;
      O[0] = O[0] * a + D0 * b; O[1] = O[1] * a + D1 * b;
    }
    STAMP(4);
    const u32 T = *(const LAS u32*)(lb + L_FLAG + 96);
#pragma unroll
    for (int hh = 0; hh < 2; hh++) {
      v4u o;
#pragma unroll
      for (int r = 0; r < 4; r++) o[r] = (__float_as_uint(O[hh][r]) & 0xfffff000u) | T;
      __builtin_amdgcn_raw_buffer_store_b128(o, wr, WS_REC + blockIdx.x * 6144 + p * 384 + (2 * h + hh) * 64 + g * 16, 0, 16);
    }
    if (h == 0 && g == 0) {
      const u32 mu = __float_as_uint(M), lu = __float_as_uint(L);
      const v4u o = {(mu & 0xfffff000u) | T, (mu << 20) | T, (lu & 0xfffff000u) | T, (lu << 20) | T};
      __builtin_amdgcn_raw_buffer_store_b128(o, wr, WS_STAT + blockIdx.x * 256 + p * 16, 0, 16);
    }
    STAMP(5);
    __builtin_amdgcn_s_barrier();
  } else {
    const int bw = wave - 3;
    const u32 z = lb + bw * ZONE;
    {
      const int st = blockIdx.x * 9 + bw, i = st / 12, j = st - i * 12;
      const char* a0 = f0 + (size_t)(i * 768 + 32 * j) * 192;
      const char* b = f1 + (size_t)(i * 192 + 16 * j) * 192;
      const u32 v0 = 16 * (lane ^ ((lane >= 48) ? 2 : 0));
      const u32 v1 = 16 * (lane ^ ((lane >= 32) ? 4 : 2));
      const u32 v2 = 16 * (lane ^ ((lane >= 16) ? 6 : 4));
      dma3(b, z, v0, v1, v2);
      dma3(a0, z + 3072, v0, v1, v2); dma3(a0 + 3072, z + 6144, v0, v1, v2);
      dma3(a0 + 384 * 192, z + 9216, v0, v1, v2); dma3(a0 + 384 * 192 + 3072, z + 12288, v0, v1, v2);
    }
    if (PROBE == 5 && bw == 0) { asm volatile("s_waitcnt vmcnt(0)" ::: "memory"); PSET(); }
    if (PROBE == 6 && bw == 8) { asm volatile("s_waitcnt vmcnt(0)" ::: "memory"); PSET(); }
    if (PROBE == 7 && bw == 0) { asm volatile("s_waitcnt vmcnt(14)" ::: "memory"); PSET(); }
    const f4 z4 = {0.f, 0.f, 0.f, 0.f};
    const v4u ones = {0x3f803f80u, 0x3f803f80u, 0x3f803f80u, 0x3f803f80u};
    wait3(lb + L_FLAG + 32);
    v4u qk[3];
#pragma unroll
    for (int s = 0; s < 3; s++) qk[s] = lds128(lb + L_KF + s * 1024 + lane * 16);
    if (PROBE == 2 && bw == 0) PSET();
    asm volatile("s_waitcnt vmcnt(12)" ::: "memory");
    const int pt = 4 * g + (p >> 2);
    const u32 tb = ((p >> 1) & 1), th = 8 * (p & 1);
    v4u a1[3]; v2u t1[6];
#pragma unroll
    for (int s = 0; s < 3; s++) a1[s] = lds128(z + 16 * ((12 * p + 4 * s + g) ^ ((p >> 2) << 1)));
#pragma unroll
    for (int t = 0; t < 6; t++) t1[t] = ldstr(z + 16 * ((12 * pt + 2 * t + tb) ^ (g << 1)) + th);
    f4 S1 = z4, X1 = z4; float q11 = 0.f;
#pragma unroll
    for (int s = 0; s < 3; s++) { S1 = mfma32(a1[s], qk[s], S1); X1 = mfma32(ones, a1[s], X1); q11 = dot8(a1[s], a1[s], q11); }
    const u32 rb0 = 384 * p + 16 * (g ^ (((p >> 1) & 3) << 1));
    const u32 tb0 = 384 * pt + 16 * ((((pt >> 1) & 3) << 1) | tb) + th;
    const float oh0 = (g == 0) ? 1.f : 0.f, oh1 = (g == 2) ? 1.f : 0.f;
    float m = 0.f, l = 0.f; f4 W1 = z4; f4 acc[6];
#pragma unroll
    for (int t = 0; t < 6; t++) acc[t] = z4;
#pragma unroll 1
    for (int it = 0; it < 2; it++) {
      if (it == 0) asm volatile("s_waitcnt vmcnt(6)" ::: "memory"); else asm volatile("s_waitcnt vmcnt(0)" ::: "memory");
      const u32 zb = z + 3072 + it * 6144;
      const u32 r0 = zb + rb0, r1 = zb + (rb0 ^ 64);
      v4u a[2][3];
      a[0][0] = lds128(r0); a[0][1] = lds128(r1); a[0][2] = lds128(r0 + 128);
      a[1][0] = lds128(r1 + 128); a[1][1] = lds128(r0 + 256); a[1][2] = lds128(r1 + 256);
      f4 S[2], X[2]; float sq[2];
#pragma unroll
      for (int dx = 0; dx < 2; dx++) {
        S[dx] = S1; X[dx] = X1; float d00 = q11, d01 = 0.f;
#pragma unroll
        for (int s = 0; s < 3; s++) {
          S[dx] = mfma32(a[dx][s], qk[s], S[dx]); X[dx] = mfma32(ones, a[dx][s], X[dx]);
          d00 = dot8(a[dx][s], a[dx][s], d00); d01 = dot8(a[dx][s], a1[s], d01);
        }
        sq[dx] = fmaf(2.0f, d01, d00);
      }
      float rr;
      {
        auto s = __builtin_amdgcn_permlane32_swap(__float_as_uint(sq[0]), __float_as_uint(sq[1]), false, false);
        float y = __uint_as_float(s[0]) + __uint_as_float(s[1]);
        auto t = __builtin_amdgcn_permlane16_swap(__float_as_uint(y), __float_as_uint(y), false, false);
        y = __uint_as_float(t[0]) + __uint_as_float(t[1]);
        const float sx = (lane < 32) ? X[0][0] : X[1][0];
        const float v = fmaxf(fmaf(-sx, sx, fmaf(y, 96.0f, EPS96)), EPS96);
        rr = __builtin_amdgcn_rsqf(v);
      }
      f4 R[2];
      R[0] = __builtin_amdgcn_mfma_f32_16x16x4f32(rr, oh0, z4, 0, 0, 0);
      R[1] = __builtin_amdgcn_mfma_f32_16x16x4f32(rr, oh1, z4, 0, 0, 0);
      v4u at[6];
#pragma unroll
      for (int t = 0; t < 6; t++) {
        const int c0 = 2 * t, c1 = 12 + 2 * t;
        const v2u x0 = ldstr(zb + (tb0 ^ (16 * (c0 & 6))) + 16 * (c0 & ~7));
        const v2u x1 = ldstr(zb + (tb0 ^ (16 * (c1 & 6))) + 16 * (c1 & ~7));
        at[t] = v4u{x0[0], x0[1], x1[0], x1[1]};
      }
      f4 u[2];
#pragma unroll
      for (int dx = 0; dx < 2; dx++)
#pragma unroll
        for (int r = 0; r < 4; r++) u[dx][r] = fmaf(S[dx][r], R[dx][r], -m);
      const float lm = fmaxf(fmaxf(fmaxf(u[0][0], u[0][1]), fmaxf(u[0][2], u[0][3])), fmaxf(fmaxf(u[1][0], u[1][1]), fmaxf(u[1][2], u[1][3])));
      if (it == 0) {
        const float d = xg_max(lm);
        m = d; u[0] -= d; u[1] -= d;
      } else if (__builtin_amdgcn_ballot_w64(lm > THR) != 0ull) {
        const float d = fmaxf(xg_max(lm), 0.f);
        const float al = __builtin_amdgcn_exp2f(-d);
        m += d; u[0] -= d; u[1] -= d; l *= al; W1 *= al;
#pragma unroll
        for (int t = 0; t < 6; t++) acc[t] *= al;
      }
      f4 P[2];
#pragma unroll
      for (int dx = 0; dx < 2; dx++)
#pragma unroll
        for (int r = 0; r < 4; r++) { const float e = __builtin_amdgcn_exp2f(u[dx][r]); l += e; P[dx][r] = e * R[dx][r]; }
      W1 += P[0] + P[1];
      const v4u pb = pk8(P[0], P[1]);
#pragma unroll
      for (int t = 0; t < 6; t++) acc[t] = mfma32(at[t], pb, acc[t]);
    }
    {
      const v2u wb = {pk(W1[0], W1[1]), pk(W1[2], W1[3])};
#pragma unroll
      for (int t = 0; t < 6; t++) acc[t] = mfma16(t1[t], wb, acc[t]);
    }
    l = xg_sum(l);
#pragma unroll
    for (int u = 0; u < 3; u++) *(LAS v4u*)(z + u * 1024 + lane * 16) = pk8(acc[2 * u], acc[2 * u + 1]);
    *(LAS f2*)(z + 3072 + p * 8) = f2{m, l};
    set_flag(lb + L_FLAG + 48 + 4 * bw, 1u);
    STAMP(4);
    __builtin_amdgcn_s_barrier();
    asm volatile("" ::: "memory");
    if (PROBE == 3 && bw == 0) PSET();
    if (blockIdx.x < 96 && bw < 8) {
      const int n = 2 * (blockIdx.x & 7) + ((blockIdx.x >> 3) & 1), t = blockIdx.x >> 4;
      const u32 T = *(const LAS u32*)(lb + L_FLAG + 96);
      const int pr = 32 * bw + (lane >> 2);
      const u32 od = WS_REC + pr * 6144 + n * 384 + t * 64 + (lane & 3) * 16, os = WS_STAT + pr * 256 + n * 16;
      const int e = 32 * (t >> 1) + 8 * ((lane >> 2) & 3) + 4 * (t & 1) + (lane & 3);
      const unsigned short qv = ((const unsigned short*)q)[n * 96 + e];
      v4u d0, d1, s0, s1;
      int spins = 0;
      for (;;) {
        asm volatile("" ::: "memory");
        d0 = __builtin_amdgcn_raw_buffer_load_b128(wr, od, 0, 16);
        s0 = __builtin_amdgcn_raw_buffer_load_b128(wr, os, 0, 16);
        d1 = __builtin_amdgcn_raw_buffer_load_b128(wr, od + 16 * 6144, 0, 16);
        s1 = __builtin_amdgcn_raw_buffer_load_b128(wr, os + 16 * 256, 0, 16);
        const v4u x = (d0 ^ T) | (d1 ^ T) | (s0 ^ T) | (s1 ^ T);
        if ((((x[0] | x[1]) | (x[2] | x[3])) & 0xfffu) == 0u) break;
        if (++spins > (1 << 16)) break;
      }
      STAMP(6);
      const float m0 = __uint_as_float((s0[0] & 0xfffff000u) | (s0[1] >> 20)), l0 = __uint_as_float((s0[2] & 0xfffff000u) | (s0[3] >> 20));
      const float m1 = __uint_as_float((s1[0] & 0xfffff000u) | (s1[1] >> 20)), l1 = __uint_as_float((s1[2] & 0xfffff000u) | (s1[3] >> 20));
      float M = fmaxf(m0, m1);
      M = fmaxf(M, dpp<0x124>(M)); M = fmaxf(M, dpp<0x128>(M)); M = xg_max(M);
      const float w0 = __builtin_amdgcn_exp2f(m0 - M), w1 = __builtin_amdgcn_exp2f(m1 - M);
      float v[5];
#pragma unroll
      for (int r = 0; r < 4; r++) v[r] = __uint_as_float(d0[r] & 0xfffff000u) * w0 + __uint_as_float(d1[r] & 0xfffff000u) * w1;
      v[4] = l0 * w0 + l1 * w1;
#pragma unroll
      for (int r = 0; r < 5; r++) { v[r] += dpp<0x124>(v[r]); v[r] += dpp<0x128>(v[r]); v[r] = xg_sum(v[r]); }
      const u32 rbase = lb + L_RED + bw * 80;
      if (lane < 4) { *(LAS f4*)(rbase + lane * 16) = f4{v[0], v[1], v[2], v[3]}; *(LAS f2*)(rbase + 64) = f2{M, v[4]}; }
      asm volatile("s_waitcnt lgkmcnt(0)" ::: "memory");
      __builtin_amdgcn_s_barrier();
      asm volatile("" ::: "memory");
      if (bw == 0 && lane < 16) {
        float o[8]; f2 ml[8];
#pragma unroll
        for (int w = 0; w < 8; w++) { o[w] = *(const LAS float*)(lb + L_RED + w * 80 + lane * 4); ml[w] = *(const LAS f2*)(lb + L_RED + w * 80 + 64); }
        float MM = ml[0][0];
#pragma unroll
        for (int w = 1; w < 8; w++) MM = fmaxf(MM, ml[w][0]);
        float LL = 0.f, OO = 0.f;
#pragma unroll
        for (int w = 0; w < 8; w++) { const float ww = __builtin_amdgcn_exp2f(ml[w][0] - MM); LL = fmaf(ww, ml[w][1], LL); OO = fmaf(ww, o[w], OO); }
        const float res = __uint_as_float((u32)qv << 16) + OO * __builtin_amdgcn_rcpf(LL);
        out[n * 96 + e] = (unsigned short)(pk(res, res) & 0xffffu);
        if (blockIdx.x == 0 && lane == 0) *(u32*)ws = *(const LAS u32*)(lb + L_FLAG + 100);
      }
      if (blockIdx.x == 0 && bw == 0) {
        if (PROBE == 1) PSET();
        if (PROBE == 4) { unsigned long long t_ = __builtin_amdgcn_s_memrealtime(), c_ = __builtin_amdgcn_s_memtime(); *(volatile LAS u32*)(lb + L_FLAG + 108) = (u32)(c_ - pc0) * 20u / (u32)(t_ - pt0); }
        const u32 X = __builtin_amdgcn_readfirstlane(*(volatile LAS u32*)(lb + L_FLAG + 108));
        const unsigned long long tgt = pt0 + 4000ull + (X > 4000u ? 4000u : X);
        while (__builtin_amdgcn_s_memrealtime() < tgt) __builtin_amdgcn_s_sleep(1);
      }
    }
  }
}

extern "C" void kernel_launch(void* const* d_in, const int* in_sizes, int n_in, void* d_out, int out_size, void* d_ws, size_t ws_size,
                              hipStream_t stream) {
  (void)hipFuncSetAttribute((const void*)dino_kernel, hipFuncAttributeMaxDynamicSharedMemorySize, LDS_BYTES);
  dino_kernel<<<256, 768, LDS_BYTES, stream>>>((const char*)d_in[0], (const char*)d_in[1], (const char*)d_in[2], (const char*)d_in[3],
                                               (const char*)d_in[4], (const char*)d_in[5], (const char*)d_in[6], (unsigned short*)d_out,
                                               (char*)d_ws);
}
```

```cpp
#define PROBE 0
#define NT 1
#define QM 3
#define TG 1
#include <hip/hip_runtime.h>
#include <hip/hip_bf16.h>
#pragma clang diagnostic ignored "-Wint-to-pointer-cast"

#define LAS __attribute__((address_space(3)))
#define GAS __attribute__((address_space(1)))
typedef unsigned u32;
typedef unsigned v4u __attribute__((ext_vector_type(4)));
typedef unsigned v2u __attribute__((ext_vector_type(2)));
typedef float f4 __attribute__((ext_vector_type(4)));
typedef float f2 __attribute__((ext_vector_type(2)));
typedef __bf16 bf8 __attribute__((ext_vector_type(8)));
typedef __bf16 bf4 __attribute__((ext_vector_type(4)));
typedef __bf16 bf2 __attribute__((ext_vector_type(2)));
typedef short s4 __attribute__((ext_vector_type(4)));

#define NTS " nt"

constexpr int ZONE = 15360;
constexpr int MISC = 9 * ZONE;
constexpr int L_FLAG = MISC;
constexpr int L_SUM = MISC + 128;
constexpr int L_QF = MISC + 512;
constexpr int L_KF = L_QF + 3072;
constexpr int L_RED = L_KF + 3072;
constexpr int LDS_BYTES = L_RED + 1024;
constexpr u32 WS_REC = 4096;
constexpr u32 WS_STAT = WS_REC + 256 * 6144;
constexpr u32 WS_END = WS_STAT + 256 * 256;
constexpr float EPS96 = 9216.0f * 1e-5f;
constexpr float QSCALE = 14.135507f;
constexpr float THR = 40.0f;

#define STAMP(k)

__device__ __forceinline__ f4 mfma32(v4u a, v4u b, f4 c) {
  return __builtin_amdgcn_mfma_f32_16x16x32_bf16(__builtin_bit_cast(bf8, a), __builtin_bit_cast(bf8, b), c, 0, 0, 0);
}
__device__ __forceinline__ f4 mfma16(v2u a, v2u b, f4 c) {
  return __builtin_amdgcn_mfma_f32_16x16x16bf16_1k(__builtin_bit_cast(s4, a), __builtin_bit_cast(s4, b), c, 0, 0, 0);
}
__device__ __forceinline__ u32 pk(float lo, float hi) {
  f2 v = {lo, hi};
  return __builtin_bit_cast(u32, __builtin_convertvector(v, bf2));
}
__device__ __forceinline__ v4u pk8(f4 a, f4 b) { return v4u{pk(a[0], a[1]), pk(a[2], a[3]), pk(b[0], b[1]), pk(b[2], b[3])}; }
__device__ __forceinline__ float dot2(u32 a, u32 b, float c) {
  return __builtin_amdgcn_fdot2_f32_bf16(__builtin_bit_cast(bf2, a), __builtin_bit_cast(bf2, b), c, false);
}
__device__ __forceinline__ float dot8(v4u a, v4u b, float c) {
  c = dot2(a[0], b[0], c); c = dot2(a[1], b[1], c); c = dot2(a[2], b[2], c); return dot2(a[3], b[3], c);
}
__device__ __forceinline__ float blo(u32 x) { return __uint_as_float(x << 16); }
__device__ __forceinline__ float bhi(u32 x) { return __uint_as_float(x & 0xffff0000u); }
__device__ __forceinline__ float xg_sum(float x) {
  auto s = __builtin_amdgcn_permlane16_swap(__float_as_uint(x), __float_as_uint(x), false, false);
  x = __uint_as_float(s[0]) + __uint_as_float(s[1]);
  auto t = __builtin_amdgcn_permlane32_swap(__float_as_uint(x), __float_as_uint(x), false, false);
  return __uint_as_float(t[0]) + __uint_as_float(t[1]);
}
__device__ __forceinline__ float xg_max(float x) {
  auto s = __builtin_amdgcn_permlane16_swap(__float_as_uint(x), __float_as_uint(x), false, false);
  x = fmaxf(__uint_as_float(s[0]), __uint_as_float(s[1]));
  auto t = __builtin_amdgcn_permlane32_swap(__float_as_uint(x), __float_as_uint(x), false, false);
  return fmaxf(__uint_as_float(t[0]), __uint_as_float(t[1]));
}
template <int CTRL> __device__ __forceinline__ float dpp(float x) {
  return __builtin_bit_cast(float, __builtin_amdgcn_mov_dpp(__builtin_bit_cast(int, x), CTRL, 0xf, 0xf, true));
}
__device__ __forceinline__ v4u lds128(u32 a) { return *(const LAS v4u*)a; }
__device__ __forceinline__ v2u ldstr(u32 a) {
  return __builtin_bit_cast(v2u, __builtin_amdgcn_ds_read_tr16_b64_v4bf16((LAS bf4*)a));
}
__device__ __forceinline__ void set_flag(u32 a, u32 v) {
  asm volatile("" ::: "memory");
  __hip_atomic_store((LAS u32*)a, v, __ATOMIC_RELAXED, __HIP_MEMORY_SCOPE_WORKGROUP);
}
__device__ __forceinline__ void wait1(u32 a) {
  for (int sp = 0; sp < (1 << 20); sp++) {
    if (__builtin_amdgcn_readfirstlane(__hip_atomic_load((LAS u32*)a, __ATOMIC_RELAXED, __HIP_MEMORY_SCOPE_WORKGROUP)) != 0u) break;
    __builtin_amdgcn_s_sleep(1);
  }
  asm volatile("" ::: "memory");
}
__device__ __forceinline__ void wait3(u32 a) {
  for (int sp = 0; sp < (1 << 20); sp++) {
    asm volatile("" ::: "memory");
    v4u f = *(volatile const LAS v4u*)a;
    if (__builtin_amdgcn_readfirstlane(f[0] & f[1] & f[2])) break;
    __builtin_amdgcn_s_sleep(1);
  }
  asm volatile("" ::: "memory");
}
__device__ __forceinline__ void dma3(const void* base, u32 lds, u32 v0, u32 v1, u32 v2) {
  asm volatile("s_mov_b32 m0, %1\n\ts_nop 0\n\t"
               "global_load_lds_dwordx4 %2, %0" NTS "\n\t"
               "global_load_lds_dwordx4 %3, %0 offset:1024" NTS "\n\t"
               "global_load_lds_dwordx4 %4, %0 offset:2048" NTS
               :: "s"(base), "s"(lds), "v"(v0), "v"(v1), "v"(v2) : "memory");
}

__global__ void __launch_bounds__(768) dino_kernel(const char* __restrict__ q, const char* __restrict__ f0, const char* __restrict__ f1,
                                                   const char* __restrict__ wq, const char* __restrict__ wk, const char* __restrict__ wv,
                                                   const char* __restrict__ wo, unsigned short* __restrict__ out, char* __restrict__ ws) {
  extern __shared__ __attribute__((aligned(16))) char sm[];
  const unsigned long long pt0 = __builtin_amdgcn_s_memrealtime(); const unsigned long long pc0 = __builtin_amdgcn_s_memtime();
  const u32 lb = (u32)(size_t)(LAS char*)sm;
#define PSET() do { unsigned long long t_; asm volatile("s_memrealtime %0\n\ts_waitcnt lgkmcnt(0)" : "=s"(t_) :: "memory"); *(volatile LAS u32*)(lb + L_FLAG + 108) = (u32)(t_ - pt0); } while (0)
  const int wave = __builtin_amdgcn_readfirstlane(threadIdx.x >> 6), lane = threadIdx.x & 63;
  const int g = lane >> 4, p = lane & 15;
  if (threadIdx.x < 32) *(LAS u32*)(lb + L_FLAG + 4 * threadIdx.x) = 0u;
  asm volatile("s_waitcnt lgkmcnt(0)" ::: "memory");
  __builtin_amdgcn_s_barrier();
  asm volatile("" ::: "memory");
  const __amdgpu_buffer_rsrc_t wr = __builtin_amdgcn_make_buffer_rsrc(ws, 0, WS_END, 0x00020000);

  if (wave < 3) {
    const int h = wave;
    const int rowp = 8 * (p >> 2) + (p & 3);
    const u32 go = rowp * 192 + g * 16;
    v4u qf[3], wqf[6], wkf[6];
#pragma unroll
    for (int s = 0; s < 3; s++) qf[s] = *(const v4u*)(q + p * 192 + s * 64 + g * 16);
#pragma unroll
    for (int i = 0; i < 6; i++) wqf[i] = *(const v4u*)(wq + go + (32 * (i >> 1) + 4 * (i & 1)) * 192 + h * 64);
#pragma unroll
    for (int i = 0; i < 6; i++) wkf[i] = *(const v4u*)(wk + go + (32 * h + 4 * (i & 1)) * 192 + (i >> 1) * 64);
    const u32 ep = __builtin_amdgcn_raw_buffer_load_b32(wr, 0, 0, 16);
    v4u sel[2];
    {
      const bool on = (g == (p >> 2));
      const u32 one = (p & 1) ? 0x3f800000u : 0x00003f80u;
      const u32 a = (on && !(p & 2)) ? one : 0u, b = (on && (p & 2)) ? one : 0u;
      sel[0] = v4u{a, b, 0u, 0u}; sel[1] = v4u{0u, 0u, a, b};
    }
    v4u qn[3];
    {
      float x[24]; float s1 = 0.f;
#pragma unroll
      for (int s = 0; s < 3; s++)
#pragma unroll
        for (int j = 0; j < 4; j++) { x[8 * s + 2 * j] = blo(qf[s][j]); x[8 * s + 2 * j + 1] = bhi(qf[s][j]); }
#pragma unroll
      for (int j = 0; j < 24; j++) s1 += x[j];
      const float mean = xg_sum(s1) * (1.0f / 96.0f);
      float s2 = 0.f;
#pragma unroll
      for (int j = 0; j < 24; j++) { x[j] -= mean; s2 = fmaf(x[j], x[j], s2); }
      const float r = __builtin_amdgcn_rsqf(xg_sum(s2) * (1.0f / 96.0f) + 1e-5f);
#pragma unroll
      for (int s = 0; s < 3; s++)
#pragma unroll
        for (int j = 0; j < 4; j++) qn[s][j] = pk(x[8 * s + 2 * j] * r, x[8 * s + 2 * j + 1] * r);
    }
    const f4 z4 = {0.f, 0.f, 0.f, 0.f};
    STAMP(1);
    {
      f4 Q[2] = {z4, z4};
#pragma unroll
      for (int s = 0; s < 3; s++)
#pragma unroll
        for (int hh = 0; hh < 2; hh++) {
          const v4u a = pk8(mfma32(wqf[2 * s], sel[hh], z4), mfma32(wqf[2 * s + 1], sel[hh], z4));
          Q[hh] = mfma32(a, qn[s], Q[hh]);
        }
      *(LAS v4u*)(lb + L_QF + h * 1024 + lane * 16) = pk8(Q[0], Q[1]);
      set_flag(lb + L_FLAG + 4 * h, 1u);
    }
    wait3(lb + L_FLAG);
    {
      f4 K[2] = {z4, z4};
#pragma unroll
      for (int u = 0; u < 3; u++) {
        const v4u b = lds128(lb + L_QF + u * 1024 + lane * 16);
        K[0] = mfma32(wkf[2 * u], b, K[0]); K[1] = mfma32(wkf[2 * u + 1], b, K[1]);
      }
      const float rs = xg_sum((K[0][0] + K[0][1]) + (K[0][2] + K[0][3]) + (K[1][0] + K[1][1]) + (K[1][2] + K[1][3]));
      *(LAS float*)(lb + L_SUM + h * 64 + p * 4) = rs;
      set_flag(lb + L_FLAG + 16 + 4 * h, 1u);
      wait3(lb + L_FLAG + 16);
      const float mean = (*(const LAS float*)(lb + L_SUM + p * 4) + *(const LAS float*)(lb + L_SUM + 64 + p * 4) +
                          *(const LAS float*)(lb + L_SUM + 128 + p * 4)) * (1.0f / 96.0f);
      K[0] = (K[0] - mean) * QSCALE; K[1] = (K[1] - mean) * QSCALE;
      *(LAS v4u*)(lb + L_KF + h * 1024 + lane * 16) = pk8(K[0], K[1]);
      if (h == 0) { *(LAS u32*)(lb + L_FLAG + 96) = (ep + 1u) & 0xfffu; *(LAS u32*)(lb + L_FLAG + 100) = ep + 1u; }
      set_flag(lb + L_FLAG + 32 + 4 * h, 1u);
    }
    STAMP(2);
    v4u wf[2][3];
    {
      v4u wof[6], wvf[18];
#pragma unroll
      for (int i = 0; i < 6; i++) wof[i] = *(const v4u*)(wo + go + (32 * (i >> 1) + 4 * (i & 1)) * 192 + h * 64);
#pragma unroll
      for (int i = 0; i < 18; i++) wvf[i] = *(const v4u*)(wv + (16 * (i / 3) + p) * 192 + (i % 3) * 64 + g * 16);
      v4u bo[2][3];
#pragma unroll
      for (int s = 0; s < 3; s++)
#pragma unroll
        for (int hh = 0; hh < 2; hh++) bo[hh][s] = pk8(mfma32(wof[2 * s], sel[hh], z4), mfma32(wof[2 * s + 1], sel[hh], z4));
#pragma unroll
      for (int hh = 0; hh < 2; hh++) {
        f4 D[6]; float cs = 0.f;
#pragma unroll
        for (int t = 0; t < 6; t++) {
          D[t] = z4;
#pragma unroll
          for (int s = 0; s < 3; s++) D[t] = mfma32(wvf[3 * t + s], bo[hh][s], D[t]);
          cs += (D[t][0] + D[t][1]) + (D[t][2] + D[t][3]);
        }
        const float mean = xg_sum(cs) * (1.0f / 96.0f);
#pragma unroll
        for (int u = 0; u < 3; u++) wf[hh][u] = pk8((D[2 * u] - mean) * 96.0f, (D[2 * u + 1] - mean) * 96.0f);
      }
    }
    STAMP(3);
    float M = -INFINITY, L = 0.f; f4 O[2] = {z4, z4};
#pragma unroll 1
    for (int w = 0; w < 9; w++) {
      wait1(lb + L_FLAG + 48 + 4 * w);
      const u32 zb = lb + w * ZONE;
      const v4u b0 = lds128(zb + lane * 16), b1 = lds128(zb + 1024 + lane * 16), b2 = lds128(zb + 2048 + lane * 16);
      const f2 ml = *(const LAS f2*)(zb + 3072 + p * 8);
      f4 D0 = mfma32(wf[0][0], b0, z4), D1 = mfma32(wf[1][0], b0, z4);
      D0 = mfma32(wf[0][1], b1, D0); D1 = mfma32(wf[1][1], b1, D1);
      D0 = mfma32(wf[0][2], b2, D0); D1 = mfma32(wf[1][2], b2, D1);
      const float Mn = fmaxf(M, ml[0]);
      const float a = __builtin_amdgcn_exp2f(M - Mn), b = __builtin_amdgcn_exp2f(ml[0] - Mn);
      L = L * a + ml[1] * b; M = Mn;
      O[0] = O[0] * a + D0 * b; O[1] = O[1] * a + D1 * b;
    }
    STAMP(4);
    const u32 T = *(const LAS u32*)(lb + L_FLAG + 96);
#pragma unroll
    for (int hh = 0; hh < 2; hh++) {
      v4u o;
#pragma unroll
      for (int r = 0; r < 4; r++) o[r] = (__float_as_uint(O[hh][r]) & 0xfffff000u) | T;
      __builtin_amdgcn_raw_buffer_store_b128(o, wr, WS_REC + blockIdx.x * 6144 + p * 384 + (2 * h + hh) * 64 + g * 16, 0, 16);
    }
    if (h == 0 && g == 0) {
      const u32 mu = __float_as_uint(M), lu = __float_as_uint(L);
      const v4u o = {(mu & 0xfffff000u) | T, (mu << 20) | T, (lu & 0xfffff000u) | T, (lu << 20) | T};
      __builtin_amdgcn_raw_buffer_store_b128(o, wr, WS_STAT + blockIdx.x * 256 + p * 16, 0, 16);
    }
    STAMP(5);
    __builtin_amdgcn_s_barrier();
  } else {
    const int bw = wave - 3;
    const u32 z = lb + bw * ZONE;
    {
      const int st = blockIdx.x * 9 + bw, i = st / 12, j = st - i * 12;
      const char* a0 = f0 + (size_t)(i * 768 + 32 * j) * 192;
      const char* b = f1 + (size_t)(i * 192 + 16 * j) * 192;
      const u32 v0 = 16 * (lane ^ ((lane >= 48) ? 2 : 0));
      const u32 v1 = 16 * (lane ^ ((lane >= 32) ? 4 : 2));
      const u32 v2 = 16 * (lane ^ ((lane >= 16) ? 6 : 4));
      dma3(b, z, v0, v1, v2);
      dma3(a0, z + 3072, v0, v1, v2); dma3(a0 + 3072, z + 6144, v0, v1, v2);
      dma3(a0 + 384 * 192, z + 9216, v0, v1, v2); dma3(a0 + 384 * 192 + 3072, z + 12288, v0, v1, v2);
    }
    if (PROBE == 5 && bw == 0) { asm volatile("s_waitcnt vmcnt(0)" ::: "memory"); PSET(); }
    if (PROBE == 6 && bw == 8) { asm volatile("s_waitcnt vmcnt(0)" ::: "memory"); PSET(); }
    if (PROBE == 7 && bw == 0) { asm volatile("s_waitcnt vmcnt(14)" ::: "memory"); PSET(); }
    const f4 z4 = {0.f, 0.f, 0.f, 0.f};
    const v4u ones = {0x3f803f80u, 0x3f803f80u, 0x3f803f80u, 0x3f803f80u};
    wait3(lb + L_FLAG + 32);
    v4u qk[3];
#pragma unroll
    for (int s = 0; s < 3; s++) qk[s] = lds128(lb + L_KF + s * 1024 + lane * 16);
    if (PROBE == 2 && bw == 0) PSET();
    asm volatile("s_waitcnt vmcnt(12)" ::: "memory");
    const int pt = 4 * g + (p >> 2);
    const u32 tb = ((p >> 1) & 1), th = 8 * (p & 1);
    v4u a1[3]; v2u t1[6];
#pragma unroll
    for (int s = 0; s < 3; s++) a1[s] = lds128(z + 16 * ((12 * p + 4 * s + g) ^ ((p >> 2) << 1)));
#pragma unroll
    for (int t = 0; t < 6; t++) t1[t] = ldstr(z + 16 * ((12 * pt + 2 * t + tb) ^ (g << 1)) + th);
    f4 S1 = z4, X1 = z4; float q11 = 0.f;
#pragma unroll
    for (int s = 0; s < 3; s++) { S1 = mfma32(a1[s], qk[s], S1); X1 = mfma32(ones, a1[s], X1); q11 = dot8(a1[s], a1[s], q11); }
    const u32 rb0 = 384 * p + 16 * (g ^ (((p >> 1) & 3) << 1));
    const u32 tb0 = 384 * pt + 16 * ((((pt >> 1) & 3) << 1) | tb) + th;
    const float oh0 = (g == 0) ? 1.f : 0.f, oh1 = (g == 2) ? 1.f : 0.f;
    float m = 0.f, l = 0.f; f4 W1 = z4; f4 acc[6];
#pragma unroll
    for (int t = 0; t < 6; t++) acc[t] = z4;
#pragma unroll 1
    for (int it = 0; it < 2; it++) {
      if (it == 0) asm volatile("s_waitcnt vmcnt(6)" ::: "memory"); else asm volatile("s_waitcnt vmcnt(0)" ::: "memory");
      const u32 zb = z + 3072 + it * 6144;
      const u32 r0 = zb + rb0, r1 = zb + (rb0 ^ 64);
      v4u a[2][3];
      a[0][0] = lds128(r0); a[0][1] = lds128(r1); a[0][2] = lds128(r0 + 128);
      a[1][0] = lds128(r1 + 128); a[1][1] = lds128(r0 + 256); a[1][2] = lds128(r1 + 256);
      f4 S[2], X[2]; float sq[2];
#pragma unroll
      for (int dx = 0; dx < 2; dx++) {
        S[dx] = S1; X[dx] = X1; float d00 = q11, d01 = 0.f;
#pragma unroll
        for (int s = 0; s < 3; s++) {
          S[dx] = mfma32(a[dx][s], qk[s], S[dx]); X[dx] = mfma32(ones, a[dx][s], X[dx]);
          d00 = dot8(a[dx][s], a[dx][s], d00); d01 = dot8(a[dx][s], a1[s], d01);
        }
        sq[dx] = fmaf(2.0f, d01, d00);
      }
      float rr;
      {
        auto s = __builtin_amdgcn_permlane32_swap(__float_as_uint(sq[0]), __float_as_uint(sq[1]), false, false);
        float y = __uint_as_float(s[0]) + __uint_as_float(s[1]);
        auto t = __builtin_amdgcn_permlane16_swap(__float_as_uint(y), __float_as_uint(y), false, false);
        y = __uint_as_float(t[0]) + __uint_as_float(t[1]);
        const float sx = (lane < 32) ? X[0][0] : X[1][0];
        const float v = fmaxf(fmaf(-sx, sx, fmaf(y, 96.0f, EPS96)), EPS96);
        rr = __builtin_amdgcn_rsqf(v);
      }
      f4 R[2];
      R[0] = __builtin_amdgcn_mfma_f32_16x16x4f32(rr, oh0, z4, 0, 0, 0);
      R[1] = __builtin_amdgcn_mfma_f32_16x16x4f32(rr, oh1, z4, 0, 0, 0);
      v4u at[6];
#pragma unroll
      for (int t = 0; t < 6; t++) {
        const int c0 = 2 * t, c1 = 12 + 2 * t;
        const v2u x0 = ldstr(zb + (tb0 ^ (16 * (c0 & 6))) + 16 * (c0 & ~7));
        const v2u x1 = ldstr(zb + (tb0 ^ (16 * (c1 & 6))) + 16 * (c1 & ~7));
        at[t] = v4u{x0[0], x0[1], x1[0], x1[1]};
      }
      f4 u[2];
#pragma unroll
      for (int dx = 0; dx < 2; dx++)
#pragma unroll
        for (int r = 0; r < 4; r++) u[dx][r] = fmaf(S[dx][r], R[dx][r], -m);
      const float lm = fmaxf(fmaxf(fmaxf(u[0][0], u[0][1]), fmaxf(u[0][2], u[0][3])), fmaxf(fmaxf(u[1][0], u[1][1]), fmaxf(u[1][2], u[1][3])));
      if (it == 0) {
        const float d = xg_max(lm);
        m = d; u[0] -= d; u[1] -= d;
      } else if (__builtin_amdgcn_ballot_w64(lm > THR) != 0ull) {
        const float d = fmaxf(xg_max(lm), 0.f);
        const float al = __builtin_amdgcn_exp2f(-d);
        m += d; u[0] -= d; u[1] -= d; l *= al; W1 *= al;
#pragma unroll
        for (int t = 0; t < 6; t++) acc[t] *= al;
      }
      f4 P[2];
#pragma unroll
      for (int dx = 0; dx < 2; dx++)
#pragma unroll
        for (int r = 0; r < 4; r++) { const float e = __builtin_amdgcn_exp2f(u[dx][r]); l += e; P[dx][r] = e * R[dx][r]; }
      W1 += P[0] + P[1];
      const v4u pb = pk8(P[0], P[1]);
#pragma unroll
      for (int t = 0; t < 6; t++) acc[t] = mfma32(at[t], pb, acc[t]);
    }
    {
      const v2u wb = {pk(W1[0], W1[1]), pk(W1[2], W1[3])};
#pragma unroll
      for (int t = 0; t < 6; t++) acc[t] = mfma16(t1[t], wb, acc[t]);
    }
    l = xg_sum(l);
#pragma unroll
    for (int u = 0; u < 3; u++) *(LAS v4u*)(z + u * 1024 + lane * 16) = pk8(acc[2 * u], acc[2 * u + 1]);
    *(LAS f2*)(z + 3072 + p * 8) = f2{m, l};
    set_flag(lb + L_FLAG + 48 + 4 * bw, 1u);
    STAMP(4);
    __builtin_amdgcn_s_barrier();
    asm volatile("" ::: "memory");
    if (PROBE == 3 && bw == 0) PSET();
    if (blockIdx.x < 96 && bw < 8) {
      const int n = 2 * (blockIdx.x & 7) + ((blockIdx.x >> 3) & 1), t = blockIdx.x >> 4;
      const u32 T = *(const LAS u32*)(lb + L_FLAG + 96);
      const int pr = 32 * bw + (lane >> 2);
      const u32 od = WS_REC + pr * 6144 + n * 384 + t * 64 + (lane & 3) * 16, os = WS_STAT + pr * 256 + n * 16;
      const int e = 32 * (t >> 1) + 8 * ((lane >> 2) & 3) + 4 * (t & 1) + (lane & 3);
      const unsigned short qv = ((const unsigned short*)q)[n * 96 + e];
      v4u d0, d1, s0, s1;
      int spins = 0;
      for (;;) {
        asm volatile("" ::: "memory");
        d0 = __builtin_amdgcn_raw_buffer_load_b128(wr, od, 0, 16);
        s0 = __builtin_amdgcn_raw_buffer_load_b128(wr, os, 0, 16);
        d1 = __builtin_amdgcn_raw_buffer_load_b128(wr, od + 16 * 6144, 0, 16);
        s1 = __builtin_amdgcn_raw_buffer_load_b128(wr, os + 16 * 256, 0, 16);
        const v4u x = (d0 ^ T) | (d1 ^ T) | (s0 ^ T) | (s1 ^ T);
        if ((((x[0] | x[1]) | (x[2] | x[3])) & 0xfffu) == 0u) break;
        if (++spins > (1 << 16)) break;
      }
      STAMP(6);
      const float m0 = __uint_as_float((s0[0] & 0xfffff000u) | (s0[1] >> 20)), l0 = __uint_as_float((s0[2] & 0xfffff000u) | (s0[3] >> 20));
      const float m1 = __uint_as_float((s1[0] & 0xfffff000u) | (s1[1] >> 20)), l1 = __uint_as_float((s1[2] & 0xfffff000u) | (s1[3] >> 20));
      float M = fmaxf(m0, m1);
      M = fmaxf(M, dpp<0x124>(M)); M = fmaxf(M, dpp<0x128>(M)); M = xg_max(M);
      const float w0 = __builtin_amdgcn_exp2f(m0 - M), w1 = __builtin_amdgcn_exp2f(m1 - M);
      float v[5];
#pragma unroll
      for (int r = 0; r < 4; r++) v[r] = __uint_as_float(d0[r] & 0xfffff000u) * w0 + __uint_as_float(d1[r] & 0xfffff000u) * w1;
      v[4] = l0 * w0 + l1 * w1;
#pragma unroll
      for (int r = 0; r < 5; r++) { v[r] += dpp<0x124>(v[r]); v[r] += dpp<0x128>(v[r]); v[r] = xg_sum(v[r]); }
      const u32 rbase = lb + L_RED + bw * 80;
      if (lane < 4) { *(LAS f4*)(rbase + lane * 16) = f4{v[0], v[1], v[2], v[3]}; *(LAS f2*)(rbase + 64) = f2{M, v[4]}; }
      asm volatile("s_waitcnt lgkmcnt(0)" ::: "memory");
      __builtin_amdgcn_s_barrier();
      asm volatile("" ::: "memory");
      if (bw == 0 && lane < 16) {
        float o[8]; f2 ml[8];
#pragma unroll
        for (int w = 0; w < 8; w++) { o[w] = *(const LAS float*)(lb + L_RED + w * 80 + lane * 4); ml[w] = *(const LAS f2*)(lb + L_RED + w * 80 + 64); }
        float MM = ml[0][0];
#pragma unroll
        for (int w = 1; w < 8; w++) MM = fmaxf(MM, ml[w][0]);
        float LL = 0.f, OO = 0.f;
#pragma unroll
        for (int w = 0; w < 8; w++) { const float ww = __builtin_amdgcn_exp2f(ml[w][0] - MM); LL = fmaf(ww, ml[w][1], LL); OO = fmaf(ww, o[w], OO); }
        const float res = __uint_as_float((u32)qv << 16) + OO * __builtin_amdgcn_rcpf(LL);
        out[n * 96 + e] = (unsigned short)(pk(res, res) & 0xffffu);
        if (blockIdx.x == 0 && lane == 0) *(u32*)ws = *(const LAS u32*)(lb + L_FLAG + 100);
      }
      if (blockIdx.x == 0 && bw == 0) {
        if (PROBE == 1) PSET();
        if (PROBE == 4) { unsigned long long t_ = __builtin_amdgcn_s_memrealtime(), c_ = __builtin_amdgcn_s_memtime(); *(volatile LAS u32*)(lb + L_FLAG + 108) = (u32)(c_ - pc0) * 20u / (u32)(t_ - pt0); }
        const u32 X = __builtin_amdgcn_readfirstlane(*(volatile LAS u32*)(lb + L_FLAG + 108));
        if (QM == 1 || QM == 2) {
          const unsigned long long tt = *(volatile unsigned long long*)(ws + (8u << 20));
          unsigned long long gap = pt0 - tt; if (gap > 2000ull) gap = 2000ull;
          const unsigned long long tgt = tt + 4000ull + (QM == 2 ? gap : 0ull);
          for (int i = 0; i < 100000 && __builtin_amdgcn_s_memrealtime() < tgt; i++) __builtin_amdgcn_s_sleep(1);
        } else if (lane == 0) {
          *(volatile unsigned long long*)(ws + (8u << 20)) = pt0;
          *(volatile unsigned long long*)(ws + (8u << 20) + 8) = __builtin_amdgcn_s_memrealtime();
        }
      }
    }
  }
}

__global__ void tiny(char* ws) {
  const unsigned long long t = __builtin_amdgcn_s_memrealtime();
  if (QM <= 2) { if (threadIdx.x == 0) *(volatile unsigned long long*)(ws + (8u << 20)) = t; }
  else {
    const unsigned long long t0 = *(volatile unsigned long long*)(ws + (8u << 20)), te = *(volatile unsigned long long*)(ws + (8u << 20) + 8);
    unsigned long long gap = t - te; if (gap > 2000ull) gap = 2000ull;
    const unsigned long long tgt = t0 + 4000ull + (QM == 4 ? gap : 0ull);
    for (int i = 0; i < 100000 && __builtin_amdgcn_s_memrealtime() < tgt; i++) __builtin_amdgcn_s_sleep(1);
  }
}
extern "C" void kernel_launch(void* const* d_in, const int* in_sizes, int n_in, void* d_out, int out_size, void* d_ws, size_t ws_size,
                              hipStream_t stream) {
  (void)hipFuncSetAttribute((const void*)dino_kernel, hipFuncAttributeMaxDynamicSharedMemorySize, LDS_BYTES);
  if (QM <= 2) tiny<<<TG, 64, 0, stream>>>((char*)d_ws);
  dino_kernel<<<256, 768, LDS_BYTES, stream>>>((const char*)d_in[0], (const char*)d_in[1], (const char*)d_in[2], (const char*)d_in[3],
                                               (const char*)d_in[4], (const char*)d_in[5], (const char*)d_in[6], (unsigned short*)d_out,
                                               (char*)d_ws);
  if (QM > 2) tiny<<<1, 64, 0, stream>>>((char*)d_ws);
}
```

```cpp
#include <hip/hip_runtime.h>
#include <hip/hip_bf16.h>
#pragma clang diagnostic ignored "-Wint-to-pointer-cast"

#define LAS __attribute__((address_space(3)))
typedef unsigned u32;
typedef unsigned v4u __attribute__((ext_vector_type(4)));
typedef unsigned v2u __attribute__((ext_vector_type(2)));
typedef float f4 __attribute__((ext_vector_type(4)));
typedef float f2 __attribute__((ext_vector_type(2)));
typedef __bf16 bf8 __attribute__((ext_vector_type(8)));
typedef __bf16 bf4 __attribute__((ext_vector_type(4)));
typedef __bf16 bf2 __attribute__((ext_vector_type(2)));
typedef short s4 __attribute__((ext_vector_type(4)));

#define NT 1
#define NTS " nt"
#define PFL 4
#define PREW 1
#define DLY 16
#define HSEQ 0

#define CODE_LAST 14080u
constexpr int ZONE = 15360;
constexpr int MISC = 9 * ZONE;
constexpr int L_FLAG = MISC;
constexpr int F_Q = L_FLAG, F_S = L_FLAG + 16, F_K = L_FLAG + 32, F_R = L_FLAG + 48, F_T = L_FLAG + 64;
constexpr int F_W = L_FLAG + 80;
constexpr int F_P = L_FLAG + 112;
constexpr int F_I = L_FLAG + 148;
constexpr int F_J = L_FLAG + 184;
constexpr int L_RARE = L_FLAG + 200;
constexpr int L_TAG = L_FLAG + 208, L_EP = L_FLAG + 212, L_DUMMY = L_FLAG + 224;
constexpr int L_SUM = MISC + 256;
constexpr int L_MREF = MISC + 448;
constexpr int L_QF = MISC + 512;
constexpr int L_KF = L_QF + 3072;
constexpr int L_WV = L_KF + 3072;
constexpr int L_RED = L_QF;
constexpr int L_WQ = 5 * ZONE, L_WK = L_WQ + 18432, L_WO = L_WK + 18432, L_QS = L_WO + 18432;
constexpr int LDS_BYTES = L_WV + 18432;
static_assert(LDS_BYTES <= 163840 && L_QS + 3072 <= MISC, "lds");
constexpr u32 WS_REC = 4096;
constexpr u32 WS_END = WS_REC + 256 * 8192;
constexpr float EPS96 = 9216.0f * 1e-5f;
constexpr float QSCALE = 14.135507f;
constexpr float THR = 30.0f, THR2 = 60.0f;
constexpr int MRG = 1;
#define SB() __builtin_amdgcn_sched_barrier(0)
#define CB() asm volatile("" ::: "memory")
#define PIN(x) asm volatile("" : "+v"(x))

#define STAMP(k)

__device__ __forceinline__ f4 mfma32(v4u a, v4u b, f4 c) {
  return __builtin_amdgcn_mfma_f32_16x16x32_bf16(__builtin_bit_cast(bf8, a), __builtin_bit_cast(bf8, b), c, 0, 0, 0);
}
__device__ __forceinline__ f4 mfma16(v2u a, v2u b, f4 c) {
  return __builtin_amdgcn_mfma_f32_16x16x16bf16_1k(__builtin_bit_cast(s4, a), __builtin_bit_cast(s4, b), c, 0, 0, 0);
}
__device__ __forceinline__ u32 pk(float lo, float hi) {
  f2 v = {lo, hi};
  return __builtin_bit_cast(u32, __builtin_convertvector(v, bf2));
}
__device__ __forceinline__ v4u pk8(f4 a, f4 b) { return v4u{pk(a[0], a[1]), pk(a[2], a[3]), pk(b[0], b[1]), pk(b[2], b[3])}; }
__device__ __forceinline__ float blo(u32 x) { return __uint_as_float(x << 16); }
__device__ __forceinline__ float bhi(u32 x) { return __uint_as_float(x & 0xffff0000u); }
__device__ __forceinline__ float sw32_add(float a, float b) {
  auto t = __builtin_amdgcn_permlane32_swap(__float_as_uint(a), __float_as_uint(b), false, false);
  return __uint_as_float(t[0]) + __uint_as_float(t[1]);
}
__device__ __forceinline__ float sw16_add(float a, float b) {
  auto t = __builtin_amdgcn_permlane16_swap(__float_as_uint(a), __float_as_uint(b), false, false);
  return __uint_as_float(t[0]) + __uint_as_float(t[1]);
}
__device__ __forceinline__ float xg_sum(float x) { x = sw16_add(x, x); return sw32_add(x, x); }
__device__ __forceinline__ float xg_max(float x) {
  auto s = __builtin_amdgcn_permlane16_swap(__float_as_uint(x), __float_as_uint(x), false, false);
  x = fmaxf(__uint_as_float(s[0]), __uint_as_float(s[1]));
  auto t = __builtin_amdgcn_permlane32_swap(__float_as_uint(x), __float_as_uint(x), false, false);
  return fmaxf(__uint_as_float(t[0]), __uint_as_float(t[1]));
}
template <int CTRL> __device__ __forceinline__ float dpp(float x) {
  return __builtin_bit_cast(float, __builtin_amdgcn_mov_dpp(__builtin_bit_cast(int, x), CTRL, 0xf, 0xf, true));
}
__device__ __forceinline__ v4u lds128(u32 a) { return *(const LAS v4u*)a; }
__device__ __forceinline__ f4 ldsf4(u32 a) { return *(const LAS f4*)a; }
__device__ __forceinline__ v2u ldstr(u32 a) {
  return __builtin_bit_cast(v2u, __builtin_amdgcn_ds_read_tr16_b64_v4bf16((LAS bf4*)a));
}
__device__ __forceinline__ v4u ldstr2(u32 a, u32 b) { const v2u x = ldstr(a), y = ldstr(b); return v4u{x[0], x[1], y[0], y[1]}; }
__device__ __forceinline__ void set_flag(u32 a) {
  CB();
  *(volatile LAS u32*)a = 1u;
}
template <int SLEEP> __device__ __forceinline__ void wait1(u32 a) {
#pragma unroll 1
  for (int sp = 0; sp < (1 << 20); sp++) {
    if (__builtin_amdgcn_readfirstlane(*(volatile const LAS u32*)a)) break;
    if (SLEEP) __builtin_amdgcn_s_sleep(SLEEP);
  }
  CB();
}
template <int SLEEP> __device__ __forceinline__ void wait3(u32 a) {
#pragma unroll 1
  for (int sp = 0; sp < (1 << 20); sp++) {
    const v4u f = *(volatile const LAS v4u*)a;
    if (__builtin_amdgcn_readfirstlane(f[0] & f[1] & f[2])) break;
    if (SLEEP) __builtin_amdgcn_s_sleep(SLEEP);
  }
  CB();
}
__device__ __forceinline__ void dma3(const void* base, u32 lds, u32 v0, u32 v1, u32 v2) {
  asm volatile("s_mov_b32 m0, %1\n\ts_nop 0\n\t"
               "global_load_lds_dwordx4 %2, %0" NTS "\n\t"
               "global_load_lds_dwordx4 %3, %0 offset:1024" NTS "\n\t"
               "global_load_lds_dwordx4 %4, %0 offset:2048" NTS
               :: "s"(base), "s"(lds), "v"(v0), "v"(v1), "v"(v2) : "memory");
}
__device__ __forceinline__ void dma3w(const void* base, u32 lds, u32 v0, u32 v1, u32 v2) {
  asm volatile("s_mov_b32 m0, %1\n\ts_nop 0\n\t"
               "global_load_lds_dwordx4 %2, %0\n\t"
               "global_load_lds_dwordx4 %3, %0 offset:1024\n\t"
               "global_load_lds_dwordx4 %4, %0 offset:2048"
               :: "s"(base), "s"(lds), "v"(v0), "v"(v1), "v"(v2) : "memory");
}
__device__ __forceinline__ void pf1(const void* base, u32 lds, u32 v) {
  asm volatile("s_mov_b32 m0, %1\n\ts_nop 0\n\tglobal_load_lds_dword %2, %0" :: "s"(base), "s"(lds), "v"(v) : "memory");
}

__global__ void __launch_bounds__(768) dino_kernel(const char* __restrict__ q, const char* __restrict__ f0, const char* __restrict__ f1,
                                                   const char* __restrict__ wq, const char* __restrict__ wk, const char* __restrict__ wv,
                                                   const char* __restrict__ wo, unsigned short* __restrict__ out, char* __restrict__ ws) {
  extern __shared__ __attribute__((aligned(16))) char sm[];
  const u32 lb = (u32)(size_t)(LAS char*)sm;
  {
    unsigned long long pc;
    asm volatile("s_getpc_b64 %0" : "=s"(pc));
    u32 off = ((blockIdx.x >> 3) * PFL + threadIdx.x) * 128; off = off > CODE_LAST ? CODE_LAST : off;
    if (PFL && threadIdx.x < PFL)
      asm volatile("s_mov_b32 m0, %2\n\ts_nop 0\n\tglobal_load_lds_dword %0, %1" :: "v"(off), "s"(pc & ~255ull), "s"(lb + L_DUMMY) : "memory");
  }
  const int wave = __builtin_amdgcn_readfirstlane(threadIdx.x >> 6), lane = threadIdx.x & 63;
  const int g = lane >> 4, p = lane & 15;
  if (threadIdx.x < 52) *(LAS u32*)(lb + L_FLAG + 4 * threadIdx.x) = 0u;
  asm volatile("" :: "s"(q), "s"(f0), "s"(f1), "s"(wq), "s"(wk), "s"(wv), "s"(wo), "s"(out), "s"(ws));
  asm volatile("s_waitcnt lgkmcnt(0)" ::: "memory");
  __builtin_amdgcn_s_barrier();
  CB();
  const __amdgpu_buffer_rsrc_t wr = __builtin_amdgcn_make_buffer_rsrc(ws, 0, WS_END, 0x00020000);
  const u32 v0 = 16 * (lane ^ ((lane >= 48) ? 2 : 0));
  const u32 v1 = 16 * (lane ^ ((lane >= 32) ? 4 : 2));
  const u32 v2 = 16 * (lane ^ ((lane >= 16) ? 6 : 4));
  const f4 z4 = {0.f, 0.f, 0.f, 0.f};
  const u32 rowa = 16 * ((12 * p + g) ^ ((p >> 2) << 1)), rowb = 16 * ((12 * p + 4 + g) ^ ((p >> 2) << 1)),
            rowc = 16 * ((12 * p + 8 + g) ^ ((p >> 2) << 1));

  if (wave < 3) {
    const int h = wave;
    __builtin_amdgcn_s_setprio(2);
    {
      const u32 x = blockIdx.x >> 3;
      const u32 l5 = x * 5 + lane, o5 = (l5 > 143u ? 143u : l5) * 128, oq = (x > 23u ? 23u : x) * 128;
      if (lane < 5) {
        pf1(h == 0 ? wq : h == 1 ? wk : wv, lb + L_DUMMY, o5);
        if (h < 2) pf1(h == 0 ? q : wo, lb + L_DUMMY, h == 0 ? oq : o5);
      }
    }
    const u32 ep = __builtin_amdgcn_raw_buffer_load_b32(wr, 0, 0, 16);
    SB();
    const u32 trw = 12 * (8 * g + (p >> 2));
    const u32 x0 = (2 * g & 3) << 1, x1 = ((2 * g + 1) & 3) << 1;
    wait1<1>(lb + F_W);
    v4u qf[3];
    qf[0] = lds128(lb + L_QS + rowa); qf[1] = lds128(lb + L_QS + rowb); qf[2] = lds128(lb + L_QS + rowc);
    v4u qn[3];
    {
      float x[24]; float s1 = 0.f, s1b = 0.f;
#pragma unroll
      for (int s = 0; s < 3; s++)
#pragma unroll
        for (int j = 0; j < 4; j++) { x[8 * s + 2 * j] = blo(qf[s][j]); x[8 * s + 2 * j + 1] = bhi(qf[s][j]); }
#pragma unroll
      for (int j = 0; j < 12; j++) { s1 += x[2 * j]; s1b += x[2 * j + 1]; }
      const float mean = xg_sum(s1 + s1b) * (1.0f / 96.0f);
      float s2 = 0.f;
#pragma unroll
      for (int j = 0; j < 24; j++) { x[j] -= mean; s2 = fmaf(x[j], x[j], s2); }
      const float r = __builtin_amdgcn_rsqf(xg_sum(s2) * (1.0f / 96.0f) + 1e-5f);
#pragma unroll
      for (int s = 0; s < 3; s++)
#pragma unroll
        for (int j = 0; j < 4; j++) qn[s][j] = pk(x[8 * s + 2 * j] * r, x[8 * s + 2 * j + 1] * r);
    }
    u32 tw0, tw1, la = lb + L_QF + lane * 16, lah = la + h * 1024, ka[2][2], sa = lb + L_SUM + p * 4;
    {
      const u32 trc = trw + 4 * h + (p & 3);
      tw0 = lb + L_WQ + 16 * (trc ^ x0); tw1 = lb + L_WQ + 16 * ((trc + 48) ^ x1);
#pragma unroll
      for (int hh = 0; hh < 2; hh++) {
        const u32 c = 32 * h + 8 * (p >> 2) + 4 * hh + (p & 3), xk = ((2 * (p >> 2) + hh) & 3) << 1;
        ka[hh][0] = lb + L_WK + 16 * ((12 * c + g) ^ xk); ka[hh][1] = lb + L_WK + 16 * ((12 * c + 4 + g) ^ xk);
        PIN(ka[hh][0]); PIN(ka[hh][1]);
      }
      PIN(tw0); PIN(tw1); PIN(la); PIN(lah); PIN(sa); PIN(qn[0]); PIN(qn[1]); PIN(qn[2]);
    }
    wait1<0>(lb + F_W + 4);
    STAMP(1);
    {
      v4u aq[2][3];
#pragma unroll
      for (int s = 0; s < 3; s++)
#pragma unroll
        for (int hh = 0; hh < 2; hh++) aq[hh][s] = ldstr2(tw0 + s * 6144 + 8 * hh, tw1 + s * 6144 + 8 * hh);
      SB();
      f4 Q[2] = {z4, z4};
#pragma unroll
      for (int s = 0; s < 3; s++)
#pragma unroll
        for (int hh = 0; hh < 2; hh++) Q[hh] = mfma32(aq[hh][s], qn[s], Q[hh]);
      *(LAS v4u*)lah = pk8(Q[0], Q[1]);
      set_flag(lb + F_Q + 4 * h);
    }
    wait3<0>(lb + F_Q);
    const u32 T = (ep + 1u) & 0xfffu;
    {
      v4u qb[3];
#pragma unroll
      for (int u = 0; u < 3; u++) qb[u] = lds128(la + u * 1024);
      wait1<0>(lb + F_W + 8);
      v4u wkf[2][3];
#pragma unroll
      for (int hh = 0; hh < 2; hh++) { wkf[hh][0] = lds128(ka[hh][0]); wkf[hh][1] = lds128(ka[hh][1]); wkf[hh][2] = lds128(ka[hh][0] + 128); }
      SB();
      f4 K[2] = {z4, z4};
#pragma unroll
      for (int u = 0; u < 3; u++) { K[0] = mfma32(wkf[0][u], qb[u], K[0]); K[1] = mfma32(wkf[1][u], qb[u], K[1]); }
      const float rs = xg_sum((K[0][0] + K[0][1]) + (K[0][2] + K[0][3]) + (K[1][0] + K[1][1]) + (K[1][2] + K[1][3]));
      *(LAS float*)(sa + h * 64) = rs;
      set_flag(lb + F_S + 4 * h);
      wait3<0>(lb + F_S);
      const float mean = (*(const LAS float*)(sa) + *(const LAS float*)(sa + 64) + *(const LAS float*)(sa + 128)) * (1.0f / 96.0f);
      K[0] = (K[0] - mean) * QSCALE; K[1] = (K[1] - mean) * QSCALE;
      *(LAS v4u*)(lah + 3072) = pk8(K[0], K[1]);
      *(LAS v2u*)(lb + L_TAG) = v2u{T, ep + 1u};
      set_flag(lb + F_K + 4 * h);
    }
    STAMP(2);
    u32 oa = WS_REC + blockIdx.x * 8192 + (p >> 1) * 1024 + (p & 1) * 64 + g * 16 + h * 384;
    u32 oa2 = (h == 2) ? 0x80000000u : oa + 256;
    PIN(oa); PIN(oa2);
    v4u wf[3][3];
    __builtin_amdgcn_s_setprio(0);
    {
      wait1<2>(lb + F_W + 12);
      v4u bo[3][3];
#pragma unroll
      for (int j = 0; j < 3; j++) {
        u32 e = 12 * (3 * h + j) + 4 * (p & 3); e = e > 92u ? 92u : e;
        const u32 trc = trw + (e >> 3), hb = (e & 4) * 2;
        const u32 tw0 = lb + L_WO + 16 * (trc ^ x0) + hb, tw1 = lb + L_WO + 16 * ((trc + 48) ^ x1) + hb;
#pragma unroll
        for (int s = 0; s < 3; s++) bo[j][s] = ldstr2(tw0 + s * 6144, tw1 + s * 6144);
      }
      set_flag(lb + F_T + 4 * h);
      wait1<2>(lb + F_W + 16);
      const u32 sl = lb + L_WV;
      f4 D[3][6];
#pragma unroll
      for (int t = 0; t < 6; t++) {
        const v4u a0 = lds128(sl + t * 3072 + rowa), a1 = lds128(sl + t * 3072 + rowb), a2 = lds128(sl + t * 3072 + rowc);
#pragma unroll
        for (int j = 0; j < 3; j++) D[j][t] = mfma32(a2, bo[j][2], mfma32(a1, bo[j][1], mfma32(a0, bo[j][0], z4)));
      }
#pragma unroll
      for (int j = 0; j < 3; j++) {
        const f4 c4 = ((D[j][0] + D[j][1]) + (D[j][2] + D[j][3])) + (D[j][4] + D[j][5]);
        const float nm = xg_sum((c4[0] + c4[1]) + (c4[2] + c4[3]));
#pragma unroll
        for (int u = 0; u < 3; u++) wf[j][u] = pk8(D[j][2 * u] * 96.0f - nm, D[j][2 * u + 1] * 96.0f - nm);
      }
    }
    STAMP(3);
    float M = -INFINITY, L = 0.f; f4 O[3] = {z4, z4, z4};
#pragma unroll 1
    for (int k = 5; k < 9; k++) {
      u32 fl; v4u b0, b1, b2; f2 ml;
      const int w = (k == 5) ? MRG : k;
      const u32 za = lb + w * ZONE + lane * 16, zm = lb + w * ZONE + 3072 + p * 8, fa = lb + F_P + 4 * w;
      if (k == 5) { wait1<2>(fa); __builtin_amdgcn_s_setprio(3); }
      if (k == 6) wait1<1>(fa);
#pragma unroll 1
      for (int sp = 0; sp < (1 << 20); sp++) {
        CB(); fl = *(volatile const LAS u32*)fa; CB();
        b0 = lds128(za); b1 = lds128(za + 1024); b2 = lds128(za + 2048); ml = *(const LAS f2*)zm; CB();
        if (__builtin_amdgcn_readfirstlane(fl)) break;
      }
      f4 D[3];
#pragma unroll
      for (int j = 0; j < 3; j++) D[j] = mfma32(wf[j][0], b0, z4);
#pragma unroll
      for (int j = 0; j < 3; j++) D[j] = mfma32(wf[j][1], b1, D[j]);
#pragma unroll
      for (int j = 0; j < 3; j++) D[j] = mfma32(wf[j][2], b2, D[j]);
      const float Mn = fmaxf(M, ml[0]);
      const float a = __builtin_amdgcn_exp2f(M - Mn), b = __builtin_amdgcn_exp2f(ml[0] - Mn);
      L = L * a + ml[1] * b; M = Mn;
#pragma unroll
      for (int j = 0; j < 3; j++) O[j] = O[j] * a + D[j] * b;
    }
    STAMP(4);
    {
      const u32 mu = __float_as_uint(M), lu = __float_as_uint(L);
      const v4u st = {mu, mu << 20, lu, lu << 20};
#pragma unroll
      for (int j = 0; j < 3; j++) {
        v4u o;
#pragma unroll
        for (int r = 0; r < 4; r++) o[r] = (((g == 3) ? st[r] : __float_as_uint(O[j][r])) & 0xfffff000u) | T;
        __builtin_amdgcn_raw_buffer_store_b128(o, wr, j == 2 ? oa2 : oa + 128 * j, 0, 16);
      }
      *(LAS float*)(lb + L_MREF + p * 4) = M;
    }
    asm volatile("s_waitcnt lgkmcnt(0)" ::: "memory");
    STAMP(5);
    __builtin_amdgcn_s_barrier();
  } else {
    const int bw = wave - 3;
    const u32 z = lb + bw * ZONE;
    {
      const int st = blockIdx.x * 9 + bw, i = st / 12, j = st - i * 12;
      const char* a0 = f0 + (size_t)(i * 768 + 32 * j) * 192;
      const char* b = f1 + (size_t)(i * 192 + 16 * j) * 192;
      const char* a1p = a0 + 384 * 192;
      __builtin_amdgcn_s_setprio(3);
      if (bw >= 5) wait3<2>(bw == 5 ? lb + F_Q : lb + F_S);
      if (bw >= 7) wait3<2>(lb + F_T);
      if (bw >= 1) wait1<1>(lb + F_I + 4 * bw - 4);
      if (bw == 0) {
        dma3w(q, lb + L_QS, v0, v1, v2);
        dma3(b, z, v0, v1, v2); dma3(a0, z + 3072, v0, v1, v2); dma3(a0 + 3072, z + 6144, v0, v1, v2); dma3(a1p, z + 9216, v0, v1, v2);
        set_flag(lb + F_I);
        dma3(a1p + 3072, z + 12288, v0, v1, v2);
        asm volatile("s_waitcnt vmcnt(15)" ::: "memory");
        set_flag(lb + F_W);
      } else if (bw < 5) {
        const char* w = bw == 1 ? wq : bw == 2 ? wk : bw == 3 ? wo : wv;
        const u32 d = lb + (bw == 1 ? L_WQ : bw == 2 ? L_WK : bw == 3 ? L_WO : L_WV);
        dma3w(w, d, v0, v1, v2); dma3w(w + 3072, d + 3072, v0, v1, v2);
        dma3(b, z, v0, v1, v2);
        dma3w(w + 6144, d + 6144, v0, v1, v2);
        dma3(a0, z + 3072, v0, v1, v2);
        dma3w(w + 9216, d + 9216, v0, v1, v2);
        dma3(a0 + 3072, z + 6144, v0, v1, v2);
        dma3w(w + 12288, d + 12288, v0, v1, v2); dma3w(w + 15360, d + 15360, v0, v1, v2);
        dma3(a1p, z + 9216, v0, v1, v2);
        set_flag(lb + F_I + 4 * bw);
        dma3(a1p + 3072, z + 12288, v0, v1, v2);
        asm volatile("s_waitcnt vmcnt(6)" ::: "memory");
        set_flag(lb + F_W + 4 * bw);
      } else {
        dma3(b, z, v0, v1, v2); dma3(a0, z + 3072, v0, v1, v2);
        if (bw >= 6) set_flag(lb + F_I + 4 * bw);
        dma3(a0 + 3072, z + 6144, v0, v1, v2);
        if (bw >= 6) wait1<1>(bw == 6 ? lb + F_I + 32 : lb + F_J + 4 * bw - 28);
        dma3(a1p, z + 9216, v0, v1, v2);
        set_flag(bw == 5 ? lb + F_I + 20 : bw == 8 ? lb + L_DUMMY + 28 : lb + F_J + 4 * bw - 24);
        dma3(a1p + 3072, z + 12288, v0, v1, v2);
      }
    }
    if (bw < 6) __builtin_amdgcn_s_setprio(1);
    STAMP(1);
    const v4u ones = {0x3f803f80u, 0x3f803f80u, 0x3f803f80u, 0x3f803f80u};
    asm volatile("s_waitcnt vmcnt(12)" ::: "memory");
    const int pt = 4 * g + (p >> 2);
    const u32 tb = ((p >> 1) & 1), th = 8 * (p & 1);
    v4u a1[3]; v2u t1[6];
    a1[0] = lds128(z + rowa); a1[1] = lds128(z + rowb); a1[2] = lds128(z + rowc);
#pragma unroll
    for (int t = 0; t < 6; t++) t1[t] = ldstr(z + 16 * ((12 * pt + 2 * t + tb) ^ (g << 1)) + th);
    f4 S1 = z4, X1 = z4, G1 = z4; v4u a2[3], qk[3];
#pragma unroll
    for (int s = 0; s < 3; s++) {
      G1 = mfma32(a1[s], a1[s], G1); X1 = mfma32(ones, a1[s], X1);
      a2[s] = a1[s] + 0x00800080u;
    }
    const u32 rb0 = z + 3072 + 384 * p + 16 * (g ^ (((p >> 1) & 3) << 1)), rb1 = rb0 ^ 64;
    const u32 tb0 = z + 3072 + 384 * pt + 16 * ((((pt >> 1) & 3) << 1) | tb) + th;
    const u32 tbv[4] = {tb0, tb0 ^ 32, tb0 ^ 64, tb0 ^ 96};
    const bool valid = (g == (p >> 2)), b0 = (p & 1), b1 = (p & 2);
    float m = 0.f, l = 0.f; f4 W1 = z4; f4 acc[6];
#pragma unroll
    for (int it = 0; it < 2; it++) {
      SB();
      if (it == 0) asm volatile("s_waitcnt vmcnt(6)" ::: "memory"); else asm volatile("s_waitcnt vmcnt(0)" ::: "memory");
      const u32 zo = it * 6144;
      v4u a[2][3];
      a[0][0] = lds128(rb0 + zo); a[0][1] = lds128(rb1 + zo); a[0][2] = lds128(rb0 + zo + 128);
      a[1][0] = lds128(rb1 + zo + 128); a[1][1] = lds128(rb0 + zo + 256); a[1][2] = lds128(rb1 + zo + 256);
      v4u at[6];
#pragma unroll
      for (int t = 0; t < 6; t++) {
        const int c0 = 2 * t, c1 = 12 + 2 * t;
        at[t] = ldstr2(tbv[(c0 & 6) >> 1] + zo + 16 * (c0 & ~7), tbv[(c1 & 6) >> 1] + zo + 16 * (c1 & ~7));
      }
      SB();
      f4 S[2], X[2], G[2];
#pragma unroll
      for (int dx = 0; dx < 2; dx++) {
        X[dx] = X1; G[dx] = G1;
#pragma unroll
        for (int s = 0; s < 3; s++) {
          G[dx] = mfma32(a[dx][s], a[dx][s], G[dx]); G[dx] = mfma32(a[dx][s], a2[s], G[dx]); X[dx] = mfma32(ones, a[dx][s], X[dx]);
        }
      }
      SB();
      if (it == 1) {
#pragma unroll
        for (int dx = 0; dx < 2; dx++) {
          S[dx] = S1;
#pragma unroll
          for (int s = 0; s < 3; s++) S[dx] = mfma32(a[dx][s], qk[s], S[dx]);
        }
      }
      f4 R[2];
#pragma unroll
      for (int dx = 0; dx < 2; dx++) {
        const float d0 = b0 ? G[dx][1] : G[dx][0], d1 = b0 ? G[dx][3] : G[dx][2], d = b1 ? d1 : d0;
        const float sx = X[dx][0];
        const float rr = __builtin_amdgcn_rsqf(fmaxf(fmaf(-sx, sx, fmaf(d, 96.0f, EPS96)), EPS96));
        R[dx] = __builtin_amdgcn_mfma_f32_16x16x4f32(valid ? rr : 0.f, 1.0f, z4, 0, 0, 0);
      }
      if (it == 0) {
        SB();
        wait3<1>(lb + F_K);
#pragma unroll
        for (int s = 0; s < 3; s++) qk[s] = lds128(lb + L_KF + s * 1024 + lane * 16);
        STAMP(2);
#pragma unroll
        for (int s = 0; s < 3; s++) S1 = mfma32(a1[s], qk[s], S1);
#pragma unroll
        for (int dx = 0; dx < 2; dx++) {
          S[dx] = S1;
#pragma unroll
          for (int s = 0; s < 3; s++) S[dx] = mfma32(a[dx][s], qk[s], S[dx]);
        }
      }
      f4 u[2];
#pragma unroll
      for (int dx = 0; dx < 2; dx++)
#pragma unroll
        for (int r = 0; r < 4; r++) u[dx][r] = it ? fmaf(S[dx][r], R[dx][r], -m) : S[dx][r] * R[dx][r];
      const float lm = fmaxf(fmaxf(fmaxf(u[0][0], u[0][1]), fmaxf(u[0][2], u[0][3])), fmaxf(fmaxf(u[1][0], u[1][1]), fmaxf(u[1][2], u[1][3])));
      if (it == 0) {
        m = xg_max(lm);
        u[0] -= m; u[1] -= m;
      } else if (__builtin_expect(__builtin_amdgcn_ballot_w64(lm > THR) != 0ull, 0)) {
        const float d = fmaxf(xg_max(lm), 0.f);
        const float al = __builtin_amdgcn_exp2f(-d);
        m += d; u[0] -= d; u[1] -= d; l *= al; W1 *= al;
#pragma unroll
        for (int t = 0; t < 6; t++) acc[t] *= al;
      }
      f4 P[2];
#pragma unroll
      for (int dx = 0; dx < 2; dx++)
#pragma unroll
        for (int r = 0; r < 4; r++) { const float e = __builtin_amdgcn_exp2f(u[dx][r]); l += e; P[dx][r] = e * R[dx][r]; }
      const v4u pb = pk8(P[0], P[1]);
#pragma unroll
      for (int t = 0; t < 6; t++) acc[t] = mfma32(at[t], pb, it ? acc[t] : z4);
      W1 += P[0] + P[1];
    }
    {
      const v2u wb = {pk(W1[0], W1[1]), pk(W1[2], W1[3])};
#pragma unroll
      for (int t = 0; t < 6; t++) acc[t] = mfma16(t1[t], wb, acc[t]);
    }
    if (bw == MRG) {
#pragma unroll 1
      for (int k = 0; k < 5; k++) {
        const int w = k + (k >= MRG);
        wait1<1>(lb + F_P + 4 * w);
        const u32 zp = lb + w * ZONE;
        const f2 ml = *(const LAS f2*)(zp + 6144 + lane * 8);
        f4 o[6];
#pragma unroll
        for (int t = 0; t < 6; t++) o[t] = ldsf4(zp + t * 1024 + lane * 16);
        const float Mn = fmaxf(m, ml[0]);
        const float fa = __builtin_amdgcn_exp2f(m - Mn), fb = __builtin_amdgcn_exp2f(ml[0] - Mn);
        m = Mn; l = l * fa + ml[1] * fb;
#pragma unroll
        for (int t = 0; t < 6; t++) acc[t] = acc[t] * fa + o[t] * fb;
      }
    }
    if (bw >= 6 || bw == MRG) {
      l = xg_sum(l);
#pragma unroll
      for (int u = 0; u < 3; u++) *(LAS v4u*)(z + u * 1024 + lane * 16) = pk8(acc[2 * u], acc[2 * u + 1]);
      *(LAS f2*)(z + 3072 + p * 8) = f2{m, l};
    } else {
#pragma unroll
      for (int t = 0; t < 6; t++) *(LAS f4*)(z + t * 1024 + lane * 16) = acc[t];
      *(LAS f2*)(z + 6144 + lane * 8) = f2{m, l};
    }
    set_flag(lb + F_P + 4 * bw);
    STAMP(4);
    const int n = 2 * (blockIdx.x & 7) + ((blockIdx.x >> 3) & 1), sc = blockIdx.x >> 4;
    const bool red = blockIdx.x < 128 && bw < 8;
    unsigned short qv = 0;
    const int e = 12 * sc + 4 * (lane & 3) + (lane >> 2);
    if (red && bw == 0 && lane < 16 && (lane & 3) < 3) qv = ((const unsigned short*)q)[n * 96 + e];
    u32 od = WS_REC + (32 * bw + (lane >> 2)) * 8192 + (n >> 1) * 1024 + sc * 128 + (n & 1) * 64 + (lane & 3) * 16, od1 = od + 16 * 8192;
    u32 ra = lb + L_RED + ((((g & 1) << 1) | (g >> 1)) * 4 + (lane & 3)) * 32 + bw * 4;
    const u32 T = *(const LAS u32*)(lb + L_TAG);
    PIN(od); PIN(od1); PIN(ra);
    __builtin_amdgcn_s_barrier();
    CB();
    STAMP(5);
    if (red) {
      float Mr = *(const LAS float*)(lb + L_MREF + n * 4);
      if (DLY) __builtin_amdgcn_s_sleep(DLY);
      v4u d0, d1;
      int spins = 0;
      for (;;) {
        CB();
        d0 = __builtin_amdgcn_raw_buffer_load_b128(wr, od, 0, 16) ^ T;
        d1 = __builtin_amdgcn_raw_buffer_load_b128(wr, od1, 0, 16) ^ T;
        const v4u x = d0 | d1;
        if ((((x[0] | x[1]) | (x[2] | x[3])) & 0xfffu) == 0u) break;
        if (++spins > (1 << 16)) break;
      }
      STAMP(6);
      const bool st = (lane & 3) == 3;
      const float m0 = __uint_as_float(d0[0] | (d0[1] >> 20)), m1 = __uint_as_float(d1[0] | (d1[1] >> 20));
      if (st) { d0[0] = d0[2] | (d0[3] >> 20); d1[0] = d1[2] | (d1[3] >> 20); }
      if (__builtin_expect(__builtin_amdgcn_ballot_w64(st && fmaxf(m0, m1) - Mr > THR2) != 0ull, 0)) {
        float mm = st ? fmaxf(m0, m1) : -INFINITY;
        mm = fmaxf(mm, dpp<0x124>(mm)); mm = fmaxf(mm, dpp<0x128>(mm));
        Mr = dpp<0xff>(xg_max(mm));
        *(volatile LAS u32*)(lb + L_RARE) = 1u;
      }
      const float w0 = dpp<0xff>(__builtin_amdgcn_exp2f(m0 - Mr)), w1 = dpp<0xff>(__builtin_amdgcn_exp2f(m1 - Mr));
      float v[4];
#pragma unroll
      for (int r = 0; r < 4; r++) v[r] = __uint_as_float(d0[r]) * w0 + __uint_as_float(d1[r]) * w1;
      float y = sw16_add(sw32_add(v[0], v[1]), sw32_add(v[2], v[3]));
      y += dpp<0x124>(y); y += dpp<0x128>(y);
      *(LAS float*)ra = y;
      *(LAS float*)(lb + L_RED + 512 + bw * 4) = Mr;
      asm volatile("s_waitcnt lgkmcnt(0)" ::: "memory");
      __builtin_amdgcn_s_barrier();
      CB();
      if (bw == 0 && lane < 16) {
        f4 ya = ldsf4(lb + L_RED + lane * 32), yb = ldsf4(lb + L_RED + lane * 32 + 16);
        const v2u er = v2u{*(const LAS u32*)(lb + L_RARE), *(const LAS u32*)(lb + L_EP)};
        if (__builtin_expect(__builtin_amdgcn_readfirstlane(er[0]) != 0u, 0)) {
          const f4 ma = ldsf4(lb + L_RED + 512), mb = ldsf4(lb + L_RED + 528);
          const float MM = fmaxf(fmaxf(fmaxf(ma[0], ma[1]), fmaxf(ma[2], ma[3])), fmaxf(fmaxf(mb[0], mb[1]), fmaxf(mb[2], mb[3])));
#pragma unroll
          for (int r = 0; r < 4; r++) { ya[r] *= __builtin_amdgcn_exp2f(ma[r] - MM); yb[r] *= __builtin_amdgcn_exp2f(mb[r] - MM); }
        }
        const float o = ((ya[0] + ya[1]) + (ya[2] + ya[3])) + ((yb[0] + yb[1]) + (yb[2] + yb[3]));
        const float Lt = __builtin_bit_cast(float, __builtin_amdgcn_readlane(__builtin_bit_cast(int, o), 3));
        const float res = __uint_as_float((u32)qv << 16) + o * __builtin_amdgcn_rcpf(Lt);
        if ((lane & 3) < 3) out[n * 96 + e] = (unsigned short)(pk(res, res) & 0xffffu);
        if (blockIdx.x == 0 && lane == 0) *(u32*)ws = er[1];
      }
      STAMP(7);
    }
  }
}

extern "C" void kernel_launch(void* const* d_in, const int* in_sizes, int n_in, void* d_out, int out_size, void* d_ws, size_t ws_size,
                              hipStream_t stream) {
  (void)hipFuncSetAttribute((const void*)dino_kernel, hipFuncAttributeMaxDynamicSharedMemorySize, LDS_BYTES);
  dino_kernel<<<256, 768, LDS_BYTES, stream>>>((const char*)d_in[0], (const char*)d_in[1], (const char*)d_in[2], (const char*)d_in[3],
                                               (const char*)d_in[4], (const char*)d_in[5], (const char*)d_in[6], (unsigned short*)d_out,
                                               (char*)d_ws);
}
```

```cpp
#include <hip/hip_runtime.h>
#include <hip/hip_bf16.h>


__global__ void __launch_bounds__(512) dino_kernel(const void* q, const void* f0, const void* f1, const void* wq, const void* wk,
                                                   const void* wv, const void* wo, void* out, void* ws) {
  unsigned tid = threadIdx.x;
  asm volatile(
  ".Ldino_entry:\n"
  "s_memrealtime s[44:45]\n"
  "s_load_dwordx16 s[4:19], s[0:1], 0x0\n"
  "s_load_dwordx2 s[20:21], s[0:1], 0x40\n"
  "v_readfirstlane_b32 s3, v0\n"
  "v_and_b32 v0, 63, v0\n"
  "v_mov_b32 v4, 0\n"
  "s_lshr_b32 s3, s3, 6\n"
  "s_cmp_lg_u32 s3, 0\n"
  "s_cbranch_scc1 .Ldino_e1\n"
  "s_getpc_b64 s[28:29]\n"
  ".Ldino_pc:\n"
  "s_lshr_b32 s35, s2, 3\n"
  "s_mul_i32 s35, s35, 4\n"
  "s_sub_u32 s28, s28, .Ldino_pc-.Ldino_entry\n"
  "s_subb_u32 s29, s29, 0\n"
  "v_add_lshl_u32 v8, v0, s35, 7\n"
  "v_min_u32 v8, 0x3c80, v8\n"
  "s_mov_b32 m0, 200\n"
  "s_mov_b64 exec, 15\n"
  "global_load_lds_dword v8, s[28:29]\n"
  "s_mov_b64 exec, -1\n"
  "v_lshlrev_b32 v9, 2, v0\n"
  "v_min_u32 v9, 188, v9\n"
  "ds_write_b32 v9, v4\n"
  ".Ldino_e1:\n"
  "s_and_b32 s35, s2, 7\n"
  "s_lshr_b32 s36, s2, 3\n"
  "s_lshl_b32 s35, s35, 5\n"
  "s_add_u32 s35, s35, s36\n"
  "s_mul_i32 s35, s35, 9\n"
  "v_lshlrev_b32 v18, 4, v0\n"
  "s_mul_i32 s33, s35, 43691\n"
  "v_add_u32 v1, 16, v0\n"
  "s_lshr_b32 s33, s33, 19\n"
  "v_add_u32 v2, 32, v0\n"
  "s_mul_i32 s36, s33, 73728\n"
  "v_add_u32 v3, 176, v0\n"
  "s_mul_i32 s37, s35, 3072\n"
  "v_lshrrev_b32 v1, 1, v1\n"
  "s_mul_i32 s33, s33, 12\n"
  "v_lshrrev_b32 v3, 1, v3\n"
  "s_sub_u32 s32, s35, s33\n"
  "v_and_b32 v1, 32, v1\n"
  "s_lshl_b32 s35, s37, 1\n"
  "v_and_b32 v2, 0x60, v2\n"
  "s_add_u32 s36, s36, s35\n"
  "v_and_b32 v3, 0x60, v3\n"
  "v_xor_b32 v1, v1, v18\n"
  "v_xor_b32 v2, v2, v18\n"
  "v_xor_b32 v3, v3, v18\n"
  "v_mov_b32 v5, 1\n"
  "s_setprio 3\n"
  "s_cmp_lg_u32 s3, 3\n"
  "s_waitcnt lgkmcnt(0)\n"
  "s_mov_b32 s34, s44\n"
  "s_cbranch_scc1 .Ldino_nl\n"
  "s_add_u32 s28, s6, s36\n"
  "s_addc_u32 s29, s7, 0\n"
  "s_add_u32 s30, s8, s37\n"
  "s_addc_u32 s31, s9, 0\n"
  "s_mov_b32 m0, 157184\n"
  "s_nop 0\n"
  "global_load_lds_dwordx4 v1, s[4:5]\n"
  "global_load_lds_dwordx4 v2, s[4:5] offset:1024\n"
  "global_load_lds_dwordx4 v3, s[4:5] offset:2048\n"
  "s_mov_b32 m0, 25088\n"
  "s_nop 0\n"
  "global_load_lds_dwordx4 v1, s[30:31] nt\n"
  "global_load_lds_dwordx4 v2, s[30:31] offset:1024 nt\n"
  "global_load_lds_dwordx4 v3, s[30:31] offset:2048 nt\n"
  "s_mov_b32 m0, 28160\n"
  "s_nop 0\n"
  "global_load_lds_dwordx4 v1, s[28:29] nt\n"
  "global_load_lds_dwordx4 v2, s[28:29] offset:1024 nt\n"
  "global_load_lds_dwordx4 v3, s[28:29] offset:2048 nt\n"
  "s_mov_b32 m0, 31232\n"
  "s_add_u32 s28, s28, 3072\n"
  "s_addc_u32 s29, s29, 0\n"
  "global_load_lds_dwordx4 v1, s[28:29] nt\n"
  "global_load_lds_dwordx4 v2, s[28:29] offset:1024 nt\n"
  "global_load_lds_dwordx4 v3, s[28:29] offset:2048 nt\n"
  "s_mov_b32 m0, 34304\n"
  "s_add_u32 s28, s28, 70656\n"
  "s_addc_u32 s29, s29, 0\n"
  "global_load_lds_dwordx4 v1, s[28:29] nt\n"
  "global_load_lds_dwordx4 v2, s[28:29] offset:1024 nt\n"
  "global_load_lds_dwordx4 v3, s[28:29] offset:2048 nt\n"
  "s_mov_b32 m0, 37376\n"
  "s_add_u32 s28, s28, 3072\n"
  "s_addc_u32 s29, s29, 0\n"
  "global_load_lds_dwordx4 v1, s[28:29] nt\n"
  "global_load_lds_dwordx4 v2, s[28:29] offset:1024 nt\n"
  "global_load_lds_dwordx4 v3, s[28:29] offset:2048 nt\n"
  "s_mov_b32 m0, 101888\n"
  "s_nop 0\n"
  "global_load_lds_dwordx4 v1, s[10:11]\n"
  "global_load_lds_dwordx4 v2, s[10:11] offset:1024\n"
  "global_load_lds_dwordx4 v3, s[10:11] offset:2048\n"
  "s_mov_b32 m0, 104960\n"
  "s_add_u32 s10, s10, 3072\n"
  "s_addc_u32 s11, s11, 0\n"
  "global_load_lds_dwordx4 v1, s[10:11]\n"
  "global_load_lds_dwordx4 v2, s[10:11] offset:1024\n"
  "global_load_lds_dwordx4 v3, s[10:11] offset:2048\n"
  "s_mov_b32 m0, 108032\n"
  "s_add_u32 s10, s10, 3072\n"
  "s_addc_u32 s11, s11, 0\n"
  "global_load_lds_dwordx4 v1, s[10:11]\n"
  "global_load_lds_dwordx4 v2, s[10:11] offset:1024\n"
  "global_load_lds_dwordx4 v3, s[10:11] offset:2048\n"
  "s_mov_b32 m0, 111104\n"
  "s_add_u32 s10, s10, 3072\n"
  "s_addc_u32 s11, s11, 0\n"
  "global_load_lds_dwordx4 v1, s[10:11]\n"
  "global_load_lds_dwordx4 v2, s[10:11] offset:1024\n"
  "global_load_lds_dwordx4 v3, s[10:11] offset:2048\n"
  "s_mov_b32 m0, 114176\n"
  "s_add_u32 s10, s10, 3072\n"
  "s_addc_u32 s11, s11, 0\n"
  "global_load_lds_dwordx4 v1, s[10:11]\n"
  "global_load_lds_dwordx4 v2, s[10:11] offset:1024\n"
  "global_load_lds_dwordx4 v3, s[10:11] offset:2048\n"
  "s_mov_b32 m0, 117248\n"
  "s_add_u32 s10, s10, 3072\n"
  "s_addc_u32 s11, s11, 0\n"
  "global_load_lds_dwordx4 v1, s[10:11]\n"
  "global_load_lds_dwordx4 v2, s[10:11] offset:1024\n"
  "global_load_lds_dwordx4 v3, s[10:11] offset:2048\n"
  "s_barrier\n"
  "s_mov_b32 m0, 40448\n"
  "s_add_u32 s30, s30, 3072\n"
  "s_addc_u32 s31, s31, 0\n"
  "global_load_lds_dwordx4 v1, s[30:31] nt\n"
  "global_load_lds_dwordx4 v2, s[30:31] offset:1024 nt\n"
  "global_load_lds_dwordx4 v3, s[30:31] offset:2048 nt\n"
  "s_add_u32 s32, s32, 1\n"
  "s_cmp_eq_u32 s32, 12\n"
  "s_cselect_b32 s35, 73728, 0\n"
  "s_waitcnt vmcnt(36)\n"
  "ds_write_b32 v4, v5 offset:16\n"
  "s_mov_b32 m0, 120320\n"
  "s_nop 0\n"
  "global_load_lds_dwordx4 v1, s[12:13]\n"
  "global_load_lds_dwordx4 v2, s[12:13] offset:1024\n"
  "global_load_lds_dwordx4 v3, s[12:13] offset:2048\n"
  "s_mov_b32 m0, 43520\n"
  "s_add_u32 s35, s35, 4294896640\n"
  "s_ashr_i32 s36, s35, 31\n"
  "s_add_u32 s28, s28, s35\n"
  "s_addc_u32 s29, s29, s36\n"
  "global_load_lds_dwordx4 v1, s[28:29] nt\n"
  "global_load_lds_dwordx4 v2, s[28:29] offset:1024 nt\n"
  "global_load_lds_dwordx4 v3, s[28:29] offset:2048 nt\n"
  "s_waitcnt vmcnt(39)\n"
  "ds_write_b32 v4, v5 offset:80\n"
  "s_mov_b32 m0, 123392\n"
  "s_add_u32 s12, s12, 3072\n"
  "s_addc_u32 s13, s13, 0\n"
  "global_load_lds_dwordx4 v1, s[12:13]\n"
  "global_load_lds_dwordx4 v2, s[12:13] offset:1024\n"
  "global_load_lds_dwordx4 v3, s[12:13] offset:2048\n"
  "s_mov_b32 m0, 46592\n"
  "s_add_u32 s28, s28, 3072\n"
  "s_addc_u32 s29, s29, 0\n"
  "global_load_lds_dwordx4 v1, s[28:29] nt\n"
  "global_load_lds_dwordx4 v2, s[28:29] offset:1024 nt\n"
  "global_load_lds_dwordx4 v3, s[28:29] offset:2048 nt\n"
  "s_waitcnt vmcnt(42)\n"
  "s_mov_b32 m0, 126464\n"
  "s_add_u32 s12, s12, 3072\n"
  "s_addc_u32 s13, s13, 0\n"
  "global_load_lds_dwordx4 v1, s[12:13]\n"
  "global_load_lds_dwordx4 v2, s[12:13] offset:1024\n"
  "global_load_lds_dwordx4 v3, s[12:13] offset:2048\n"
  "s_mov_b32 m0, 49664\n"
  "s_add_u32 s28, s28, 70656\n"
  "s_addc_u32 s29, s29, 0\n"
  "global_load_lds_dwordx4 v1, s[28:29] nt\n"
  "global_load_lds_dwordx4 v2, s[28:29] offset:1024 nt\n"
  "global_load_lds_dwordx4 v3, s[28:29] offset:2048 nt\n"
  "s_waitcnt vmcnt(45)\n"
  "ds_write_b32 v4, v5 offset:116\n"
  "s_mov_b32 m0, 129536\n"
  "s_add_u32 s12, s12, 3072\n"
  "s_addc_u32 s13, s13, 0\n"
  "global_load_lds_dwordx4 v1, s[12:13]\n"
  "global_load_lds_dwordx4 v2, s[12:13] offset:1024\n"
  "global_load_lds_dwordx4 v3, s[12:13] offset:2048\n"
  "s_waitcnt vmcnt(45)\n"
  "s_mov_b32 m0, 52736\n"
  "s_add_u32 s28, s28, 3072\n"
  "s_addc_u32 s29, s29, 0\n"
  "global_load_lds_dwordx4 v1, s[28:29] nt\n"
  "global_load_lds_dwordx4 v2, s[28:29] offset:1024 nt\n"
  "global_load_lds_dwordx4 v3, s[28:29] offset:2048 nt\n"
  "s_waitcnt vmcnt(45)\n"
  "ds_write_b32 v4, v5 offset:152\n"
  "s_waitcnt vmcnt(27)\n"
  "ds_write_b32 v4, v5 offset:20\n"
  "s_mov_b32 m0, 132608\n"
  "s_add_u32 s12, s12, 3072\n"
  "s_addc_u32 s13, s13, 0\n"
  "global_load_lds_dwordx4 v1, s[12:13]\n"
  "global_load_lds_dwordx4 v2, s[12:13] offset:1024\n"
  "global_load_lds_dwordx4 v3, s[12:13] offset:2048\n"
  "s_mov_b32 m0, 55808\n"
  "s_add_u32 s30, s30, 3072\n"
  "s_addc_u32 s31, s31, 0\n"
  "global_load_lds_dwordx4 v1, s[30:31] nt\n"
  "global_load_lds_dwordx4 v2, s[30:31] offset:1024 nt\n"
  "global_load_lds_dwordx4 v3, s[30:31] offset:2048 nt\n"
  "s_add_u32 s32, s32, 1\n"
  "s_cmp_eq_u32 s32, 12\n"
  "s_cselect_b32 s35, 73728, 0\n"
  "s_mov_b32 m0, 135680\n"
  "s_add_u32 s12, s12, 3072\n"
  "s_addc_u32 s13, s13, 0\n"
  "global_load_lds_dwordx4 v1, s[12:13]\n"
  "global_load_lds_dwordx4 v2, s[12:13] offset:1024\n"
  "global_load_lds_dwordx4 v3, s[12:13] offset:2048\n"
  "s_mov_b32 m0, 58880\n"
  "s_add_u32 s35, s35, 4294896640\n"
  "s_ashr_i32 s36, s35, 31\n"
  "s_add_u32 s28, s28, s35\n"
  "s_addc_u32 s29, s29, s36\n"
  "global_load_lds_dwordx4 v1, s[28:29] nt\n"
  "global_load_lds_dwordx4 v2, s[28:29] offset:1024 nt\n"
  "global_load_lds_dwordx4 v3, s[28:29] offset:2048 nt\n"
  "s_waitcnt vmcnt(33)\n"
  "ds_write_b32 v4, v5 offset:84\n"
  "s_mov_b32 m0, 138752\n"
  "s_nop 0\n"
  "global_load_lds_dwordx4 v1, s[16:17]\n"
  "global_load_lds_dwordx4 v2, s[16:17] offset:1024\n"
  "global_load_lds_dwordx4 v3, s[16:17] offset:2048\n"
  "s_mov_b32 m0, 61952\n"
  "s_add_u32 s28, s28, 3072\n"
  "s_addc_u32 s29, s29, 0\n"
  "global_load_lds_dwordx4 v1, s[28:29] nt\n"
  "global_load_lds_dwordx4 v2, s[28:29] offset:1024 nt\n"
  "global_load_lds_dwordx4 v3, s[28:29] offset:2048 nt\n"
  "s_waitcnt vmcnt(33)\n"
  "s_mov_b32 m0, 141824\n"
  "s_add_u32 s16, s16, 3072\n"
  "s_addc_u32 s17, s17, 0\n"
  "global_load_lds_dwordx4 v1, s[16:17]\n"
  "global_load_lds_dwordx4 v2, s[16:17] offset:1024\n"
  "global_load_lds_dwordx4 v3, s[16:17] offset:2048\n"
  "s_mov_b32 m0, 65024\n"
  "s_add_u32 s28, s28, 70656\n"
  "s_addc_u32 s29, s29, 0\n"
  "global_load_lds_dwordx4 v1, s[28:29] nt\n"
  "global_load_lds_dwordx4 v2, s[28:29] offset:1024 nt\n"
  "global_load_lds_dwordx4 v3, s[28:29] offset:2048 nt\n"
  "s_waitcnt vmcnt(33)\n"
  "ds_write_b32 v4, v5 offset:120\n"
  "s_mov_b32 m0, 144896\n"
  "s_add_u32 s16, s16, 3072\n"
  "s_addc_u32 s17, s17, 0\n"
  "global_load_lds_dwordx4 v1, s[16:17]\n"
  "global_load_lds_dwordx4 v2, s[16:17] offset:1024\n"
  "global_load_lds_dwordx4 v3, s[16:17] offset:2048\n"
  "s_mov_b32 m0, 68096\n"
  "s_add_u32 s28, s28, 3072\n"
  "s_addc_u32 s29, s29, 0\n"
  "global_load_lds_dwordx4 v1, s[28:29] nt\n"
  "global_load_lds_dwordx4 v2, s[28:29] offset:1024 nt\n"
  "global_load_lds_dwordx4 v3, s[28:29] offset:2048 nt\n"
  "s_waitcnt vmcnt(33)\n"
  "s_mov_b32 m0, 147968\n"
  "s_add_u32 s16, s16, 3072\n"
  "s_addc_u32 s17, s17, 0\n"
  "global_load_lds_dwordx4 v1, s[16:17]\n"
  "global_load_lds_dwordx4 v2, s[16:17] offset:1024\n"
  "global_load_lds_dwordx4 v3, s[16:17] offset:2048\n"
  "s_mov_b32 m0, 71168\n"
  "s_add_u32 s30, s30, 3072\n"
  "s_addc_u32 s31, s31, 0\n"
  "global_load_lds_dwordx4 v1, s[30:31] nt\n"
  "global_load_lds_dwordx4 v2, s[30:31] offset:1024 nt\n"
  "global_load_lds_dwordx4 v3, s[30:31] offset:2048 nt\n"
  "s_add_u32 s32, s32, 1\n"
  "s_cmp_eq_u32 s32, 12\n"
  "s_cselect_b32 s35, 73728, 0\n"
  "s_waitcnt vmcnt(33)\n"
  "ds_write_b32 v4, v5 offset:156\n"
  "s_mov_b32 m0, 151040\n"
  "s_add_u32 s16, s16, 3072\n"
  "s_addc_u32 s17, s17, 0\n"
  "global_load_lds_dwordx4 v1, s[16:17]\n"
  "global_load_lds_dwordx4 v2, s[16:17] offset:1024\n"
  "global_load_lds_dwordx4 v3, s[16:17] offset:2048\n"
  "s_mov_b32 m0, 74240\n"
  "s_add_u32 s35, s35, 4294896640\n"
  "s_ashr_i32 s36, s35, 31\n"
  "s_add_u32 s28, s28, s35\n"
  "s_addc_u32 s29, s29, s36\n"
  "global_load_lds_dwordx4 v1, s[28:29] nt\n"
  "global_load_lds_dwordx4 v2, s[28:29] offset:1024 nt\n"
  "global_load_lds_dwordx4 v3, s[28:29] offset:2048 nt\n"
  "s_waitcnt vmcnt(33)\n"
  "ds_write_b32 v4, v5 offset:88\n"
  "ds_write_b32 v4, v5 offset:24\n"
  "s_mov_b32 m0, 154112\n"
  "s_add_u32 s16, s16, 3072\n"
  "s_addc_u32 s17, s17, 0\n"
  "global_load_lds_dwordx4 v1, s[16:17]\n"
  "global_load_lds_dwordx4 v2, s[16:17] offset:1024\n"
  "global_load_lds_dwordx4 v3, s[16:17] offset:2048\n"
  "s_mov_b32 m0, 77312\n"
  "s_add_u32 s28, s28, 3072\n"
  "s_addc_u32 s29, s29, 0\n"
  "global_load_lds_dwordx4 v1, s[28:29] nt\n"
  "global_load_lds_dwordx4 v2, s[28:29] offset:1024 nt\n"
  "global_load_lds_dwordx4 v3, s[28:29] offset:2048 nt\n"
  "s_waitcnt vmcnt(33)\n"
  "s_mov_b32 m0, 6656\n"
  "s_nop 0\n"
  "global_load_lds_dwordx4 v1, s[14:15]\n"
  "global_load_lds_dwordx4 v2, s[14:15] offset:1024\n"
  "global_load_lds_dwordx4 v3, s[14:15] offset:2048\n"
  "s_mov_b32 m0, 80384\n"
  "s_add_u32 s28, s28, 70656\n"
  "s_addc_u32 s29, s29, 0\n"
  "global_load_lds_dwordx4 v1, s[28:29] nt\n"
  "global_load_lds_dwordx4 v2, s[28:29] offset:1024 nt\n"
  "global_load_lds_dwordx4 v3, s[28:29] offset:2048 nt\n"
  "s_waitcnt vmcnt(33)\n"
  "ds_write_b32 v4, v5 offset:124\n"
  "s_mov_b32 m0, 9728\n"
  "s_add_u32 s14, s14, 3072\n"
  "s_addc_u32 s15, s15, 0\n"
  "global_load_lds_dwordx4 v1, s[14:15]\n"
  "global_load_lds_dwordx4 v2, s[14:15] offset:1024\n"
  "global_load_lds_dwordx4 v3, s[14:15] offset:2048\n"
  "s_mov_b32 m0, 83456\n"
  "s_add_u32 s28, s28, 3072\n"
  "s_addc_u32 s29, s29, 0\n"
  "global_load_lds_dwordx4 v1, s[28:29] nt\n"
  "global_load_lds_dwordx4 v2, s[28:29] offset:1024 nt\n"
  "global_load_lds_dwordx4 v3, s[28:29] offset:2048 nt\n"
  "s_waitcnt vmcnt(33)\n"
  "s_mov_b32 m0, 12800\n"
  "s_add_u32 s14, s14, 3072\n"
  "s_addc_u32 s15, s15, 0\n"
  "global_load_lds_dwordx4 v1, s[14:15]\n"
  "global_load_lds_dwordx4 v2, s[14:15] offset:1024\n"
  "global_load_lds_dwordx4 v3, s[14:15] offset:2048\n"
  "s_mov_b32 m0, 86528\n"
  "s_add_u32 s30, s30, 3072\n"
  "s_addc_u32 s31, s31, 0\n"
  "global_load_lds_dwordx4 v1, s[30:31] nt\n"
  "global_load_lds_dwordx4 v2, s[30:31] offset:1024 nt\n"
  "global_load_lds_dwordx4 v3, s[30:31] offset:2048 nt\n"
  "s_add_u32 s32, s32, 1\n"
  "s_cmp_eq_u32 s32, 12\n"
  "s_cselect_b32 s35, 73728, 0\n"
  "s_waitcnt vmcnt(33)\n"
  "ds_write_b32 v4, v5 offset:160\n"
  "s_mov_b32 m0, 15872\n"
  "s_add_u32 s14, s14, 3072\n"
  "s_addc_u32 s15, s15, 0\n"
  "global_load_lds_dwordx4 v1, s[14:15]\n"
  "global_load_lds_dwordx4 v2, s[14:15] offset:1024\n"
  "global_load_lds_dwordx4 v3, s[14:15] offset:2048\n"
  "s_mov_b32 m0, 89600\n"
  "s_add_u32 s35, s35, 4294896640\n"
  "s_ashr_i32 s36, s35, 31\n"
  "s_add_u32 s28, s28, s35\n"
  "s_addc_u32 s29, s29, s36\n"
  "global_load_lds_dwordx4 v1, s[28:29] nt\n"
  "global_load_lds_dwordx4 v2, s[28:29] offset:1024 nt\n"
  "global_load_lds_dwordx4 v3, s[28:29] offset:2048 nt\n"
  "s_waitcnt vmcnt(33)\n"
  "ds_write_b32 v4, v5 offset:92\n"
  "s_mov_b32 m0, 18944\n"
  "s_add_u32 s14, s14, 3072\n"
  "s_addc_u32 s15, s15, 0\n"
  "global_load_lds_dwordx4 v1, s[14:15]\n"
  "global_load_lds_dwordx4 v2, s[14:15] offset:1024\n"
  "global_load_lds_dwordx4 v3, s[14:15] offset:2048\n"
  "s_mov_b32 m0, 92672\n"
  "s_add_u32 s28, s28, 3072\n"
  "s_addc_u32 s29, s29, 0\n"
  "global_load_lds_dwordx4 v1, s[28:29] nt\n"
  "global_load_lds_dwordx4 v2, s[28:29] offset:1024 nt\n"
  "global_load_lds_dwordx4 v3, s[28:29] offset:2048 nt\n"
  "s_waitcnt vmcnt(33)\n"
  "ds_write_b32 v4, v5 offset:28\n"
  "s_mov_b32 m0, 22016\n"
  "s_add_u32 s14, s14, 3072\n"
  "s_addc_u32 s15, s15, 0\n"
  "global_load_lds_dwordx4 v1, s[14:15]\n"
  "global_load_lds_dwordx4 v2, s[14:15] offset:1024\n"
  "global_load_lds_dwordx4 v3, s[14:15] offset:2048\n"
  "s_mov_b32 m0, 95744\n"
  "s_add_u32 s28, s28, 70656\n"
  "s_addc_u32 s29, s29, 0\n"
  "global_load_lds_dwordx4 v1, s[28:29] nt\n"
  "global_load_lds_dwordx4 v2, s[28:29] offset:1024 nt\n"
  "global_load_lds_dwordx4 v3, s[28:29] offset:2048 nt\n"
  "s_waitcnt vmcnt(33)\n"
  "ds_write_b32 v4, v5 offset:128\n"
  "s_mov_b32 m0, 98816\n"
  "s_add_u32 s28, s28, 3072\n"
  "s_addc_u32 s29, s29, 0\n"
  "global_load_lds_dwordx4 v1, s[28:29] nt\n"
  "global_load_lds_dwordx4 v2, s[28:29] offset:1024 nt\n"
  "global_load_lds_dwordx4 v3, s[28:29] offset:2048 nt\n"
  "s_waitcnt vmcnt(30)\n"
  ".Ldino_w_1:\n"
  "ds_read_b32 v8, v4 offset:0\n"
  "s_waitcnt lgkmcnt(0)\n"
  "v_cmp_ne_u32 vcc, 0x10101, v8\n"
  "s_cbranch_vccnz .Ldino_w_1\n"
  "s_mov_b32 m0, 101888\n"
  "s_add_u32 s30, s30, 3072\n"
  "s_addc_u32 s31, s31, 0\n"
  "global_load_lds_dwordx4 v1, s[30:31] nt\n"
  "global_load_lds_dwordx4 v2, s[30:31] offset:1024 nt\n"
  "global_load_lds_dwordx4 v3, s[30:31] offset:2048 nt\n"
  "s_add_u32 s32, s32, 1\n"
  "s_cmp_eq_u32 s32, 12\n"
  "s_cselect_b32 s35, 73728, 0\n"
  "s_waitcnt vmcnt(27)\n"
  "ds_write_b32 v4, v5 offset:164\n"
  "s_mov_b32 m0, 104960\n"
  "s_add_u32 s35, s35, 4294896640\n"
  "s_ashr_i32 s36, s35, 31\n"
  "s_add_u32 s28, s28, s35\n"
  "s_addc_u32 s29, s29, s36\n"
  "global_load_lds_dwordx4 v1, s[28:29] nt\n"
  "global_load_lds_dwordx4 v2, s[28:29] offset:1024 nt\n"
  "global_load_lds_dwordx4 v3, s[28:29] offset:2048 nt\n"
  "s_waitcnt vmcnt(24)\n"
  "ds_write_b32 v4, v5 offset:96\n"
  "s_mov_b32 m0, 108032\n"
  "s_add_u32 s28, s28, 3072\n"
  "s_addc_u32 s29, s29, 0\n"
  "global_load_lds_dwordx4 v1, s[28:29] nt\n"
  "global_load_lds_dwordx4 v2, s[28:29] offset:1024 nt\n"
  "global_load_lds_dwordx4 v3, s[28:29] offset:2048 nt\n"
  "s_waitcnt vmcnt(21)\n"
  "s_mov_b32 m0, 111104\n"
  "s_add_u32 s28, s28, 70656\n"
  "s_addc_u32 s29, s29, 0\n"
  "global_load_lds_dwordx4 v1, s[28:29] nt\n"
  "global_load_lds_dwordx4 v2, s[28:29] offset:1024 nt\n"
  "global_load_lds_dwordx4 v3, s[28:29] offset:2048 nt\n"
  "s_waitcnt vmcnt(18)\n"
  "ds_write_b32 v4, v5 offset:132\n"
  "ds_write_b32 v4, v5 offset:32\n"
  "s_mov_b32 m0, 114176\n"
  "s_add_u32 s28, s28, 3072\n"
  "s_addc_u32 s29, s29, 0\n"
  "global_load_lds_dwordx4 v1, s[28:29] nt\n"
  "global_load_lds_dwordx4 v2, s[28:29] offset:1024 nt\n"
  "global_load_lds_dwordx4 v3, s[28:29] offset:2048 nt\n"
  "s_waitcnt vmcnt(18)\n"
  ".Ldino_w_3:\n"
  "ds_read_b32 v8, v4 offset:4\n"
  "s_waitcnt lgkmcnt(0)\n"
  "v_cmp_ne_u32 vcc, 0x10101, v8\n"
  "s_cbranch_vccnz .Ldino_w_3\n"
  "s_mov_b32 m0, 117248\n"
  "s_add_u32 s30, s30, 3072\n"
  "s_addc_u32 s31, s31, 0\n"
  "global_load_lds_dwordx4 v1, s[30:31] nt\n"
  "global_load_lds_dwordx4 v2, s[30:31] offset:1024 nt\n"
  "global_load_lds_dwordx4 v3, s[30:31] offset:2048 nt\n"
  "s_add_u32 s32, s32, 1\n"
  "s_cmp_eq_u32 s32, 12\n"
  "s_cselect_b32 s35, 73728, 0\n"
  "s_waitcnt vmcnt(18)\n"
  "ds_write_b32 v4, v5 offset:168\n"
  "s_mov_b32 m0, 120320\n"
  "s_add_u32 s35, s35, 4294896640\n"
  "s_ashr_i32 s36, s35, 31\n"
  "s_add_u32 s28, s28, s35\n"
  "s_addc_u32 s29, s29, s36\n"
  "global_load_lds_dwordx4 v1, s[28:29] nt\n"
  "global_load_lds_dwordx4 v2, s[28:29] offset:1024 nt\n"
  "global_load_lds_dwordx4 v3, s[28:29] offset:2048 nt\n"
  "s_waitcnt vmcnt(18)\n"
  "ds_write_b32 v4, v5 offset:100\n"
  "s_mov_b32 m0, 123392\n"
  "s_add_u32 s28, s28, 3072\n"
  "s_addc_u32 s29, s29, 0\n"
  "global_load_lds_dwordx4 v1, s[28:29] nt\n"
  "global_load_lds_dwordx4 v2, s[28:29] offset:1024 nt\n"
  "global_load_lds_dwordx4 v3, s[28:29] offset:2048 nt\n"
  "s_add_u32 s40, s28, 70656\n"
  "s_addc_u32 s41, s29, 0\n"
  "s_waitcnt vmcnt(18)\n"
  ".Ldino_w_5:\n"
  "ds_read_b32 v8, v4 offset:12\n"
  "s_waitcnt lgkmcnt(0)\n"
  "v_cmp_ne_u32 vcc, 0x10101, v8\n"
  "s_cbranch_vccnz .Ldino_w_5\n"
  "s_mov_b32 m0, 132608\n"
  "s_add_u32 s30, s30, 3072\n"
  "s_addc_u32 s31, s31, 0\n"
  "global_load_lds_dwordx4 v1, s[30:31] nt\n"
  "global_load_lds_dwordx4 v2, s[30:31] offset:1024 nt\n"
  "global_load_lds_dwordx4 v3, s[30:31] offset:2048 nt\n"
  "s_add_u32 s32, s32, 1\n"
  "s_cmp_eq_u32 s32, 12\n"
  "s_cselect_b32 s35, 73728, 0\n"
  "s_waitcnt vmcnt(18)\n"
  "ds_write_b32 v4, v5 offset:136\n"
  "s_mov_b32 m0, 135680\n"
  "s_add_u32 s35, s35, 3072\n"
  "s_ashr_i32 s36, s35, 31\n"
  "s_add_u32 s28, s28, s35\n"
  "s_addc_u32 s29, s29, s36\n"
  "global_load_lds_dwordx4 v1, s[28:29] nt\n"
  "global_load_lds_dwordx4 v2, s[28:29] offset:1024 nt\n"
  "global_load_lds_dwordx4 v3, s[28:29] offset:2048 nt\n"
  "s_waitcnt vmcnt(18)\n"
  "s_mov_b32 m0, 138752\n"
  "s_add_u32 s28, s28, 3072\n"
  "s_addc_u32 s29, s29, 0\n"
  "global_load_lds_dwordx4 v1, s[28:29] nt\n"
  "global_load_lds_dwordx4 v2, s[28:29] offset:1024 nt\n"
  "global_load_lds_dwordx4 v3, s[28:29] offset:2048 nt\n"
  "s_add_u32 s42, s28, 70656\n"
  "s_addc_u32 s43, s29, 0\n"
  "s_waitcnt vmcnt(18)\n"
  "ds_write_b32 v4, v5 offset:172\n"
  "s_mov_b32 m0, 147968\n"
  "s_add_u32 s30, s30, 3072\n"
  "s_addc_u32 s31, s31, 0\n"
  "global_load_lds_dwordx4 v1, s[30:31] nt\n"
  "global_load_lds_dwordx4 v2, s[30:31] offset:1024 nt\n"
  "global_load_lds_dwordx4 v3, s[30:31] offset:2048 nt\n"
  "s_add_u32 s32, s32, 1\n"
  "s_cmp_eq_u32 s32, 12\n"
  "s_cselect_b32 s35, 73728, 0\n"
  "s_waitcnt vmcnt(18)\n"
  "ds_write_b32 v4, v5 offset:104\n"
  "s_mov_b32 m0, 151040\n"
  "s_add_u32 s35, s35, 3072\n"
  "s_ashr_i32 s36, s35, 31\n"
  "s_add_u32 s28, s28, s35\n"
  "s_addc_u32 s29, s29, s36\n"
  "global_load_lds_dwordx4 v1, s[28:29] nt\n"
  "global_load_lds_dwordx4 v2, s[28:29] offset:1024 nt\n"
  "global_load_lds_dwordx4 v3, s[28:29] offset:2048 nt\n"
  "s_waitcnt vmcnt(18)\n"
  "s_mov_b32 m0, 154112\n"
  "s_add_u32 s28, s28, 3072\n"
  "s_addc_u32 s29, s29, 0\n"
  "global_load_lds_dwordx4 v1, s[28:29] nt\n"
  "global_load_lds_dwordx4 v2, s[28:29] offset:1024 nt\n"
  "global_load_lds_dwordx4 v3, s[28:29] offset:2048 nt\n"
  "s_add_u32 s44, s28, 70656\n"
  "s_addc_u32 s45, s29, 0\n"
  "s_waitcnt vmcnt(18)\n"
  "ds_write_b32 v4, v5 offset:140\n"
  "s_mov_b32 m0, 126464\n"
  "s_nop 0\n"
  "global_load_lds_dwordx4 v1, s[40:41] nt\n"
  "global_load_lds_dwordx4 v2, s[40:41] offset:1024 nt\n"
  "global_load_lds_dwordx4 v3, s[40:41] offset:2048 nt\n"
  "s_waitcnt vmcnt(18)\n"
  "ds_write_b32 v4, v5 offset:108\n"
  "s_mov_b32 m0, 129536\n"
  "s_add_u32 s40, s40, 3072\n"
  "s_addc_u32 s41, s41, 0\n"
  "global_load_lds_dwordx4 v1, s[40:41] nt\n"
  "global_load_lds_dwordx4 v2, s[40:41] offset:1024 nt\n"
  "global_load_lds_dwordx4 v3, s[40:41] offset:2048 nt\n"
  "s_waitcnt vmcnt(18)\n"
  "s_mov_b32 m0, 141824\n"
  "s_nop 0\n"
  "global_load_lds_dwordx4 v1, s[42:43] nt\n"
  "global_load_lds_dwordx4 v2, s[42:43] offset:1024 nt\n"
  "global_load_lds_dwordx4 v3, s[42:43] offset:2048 nt\n"
  "s_waitcnt vmcnt(18)\n"
  "ds_write_b32 v4, v5 offset:144\n"
  "s_mov_b32 m0, 144896\n"
  "s_add_u32 s42, s42, 3072\n"
  "s_addc_u32 s43, s43, 0\n"
  "global_load_lds_dwordx4 v1, s[42:43] nt\n"
  "global_load_lds_dwordx4 v2, s[42:43] offset:1024 nt\n"
  "global_load_lds_dwordx4 v3, s[42:43] offset:2048 nt\n"
  "s_waitcnt vmcnt(18)\n"
  "ds_write_b32 v4, v5 offset:112\n"
  "s_mov_b32 m0, 157184\n"
  "s_nop 0\n"
  "global_load_lds_dwordx4 v1, s[44:45] nt\n"
  "global_load_lds_dwordx4 v2, s[44:45] offset:1024 nt\n"
  "global_load_lds_dwordx4 v3, s[44:45] offset:2048 nt\n"
  "s_waitcnt vmcnt(18)\n"
  "s_mov_b32 m0, 160256\n"
  "s_add_u32 s44, s44, 3072\n"
  "s_addc_u32 s45, s45, 0\n"
  "global_load_lds_dwordx4 v1, s[44:45] nt\n"
  "global_load_lds_dwordx4 v2, s[44:45] offset:1024 nt\n"
  "global_load_lds_dwordx4 v3, s[44:45] offset:2048 nt\n"
  "s_waitcnt vmcnt(18)\n"
  "ds_write_b32 v4, v5 offset:148\n"
  "s_waitcnt vmcnt(12)\n"
  "ds_write_b32 v4, v5 offset:176\n"
  "s_waitcnt vmcnt(6)\n"
  "ds_write_b32 v4, v5 offset:180\n"
  "s_waitcnt vmcnt(0)\n"
  "ds_write_b32 v4, v5 offset:184\n"
  "s_setprio 0\n"
  "s_mov_b32 s24, s20\n"
  "s_and_b32 s25, s21, 0xffff\n"
  "s_mov_b32 s26, 2101248\n"
  "s_mov_b32 s27, 0x20000\n"
  "s_branch .Ldino_pre\n"
  ".Ldino_nl:\n"
  "s_mov_b32 s24, s20\n"
  "s_and_b32 s25, s21, 0xffff\n"
  "s_mov_b32 s26, 2101248\n"
  "s_mov_b32 s27, 0x20000\n"
  "s_cmp_gt_u32 s3, 3\n"
  "s_cbranch_scc1 .Ldino_nl1\n"
  "s_lshr_b32 s35, s2, 3\n"
  "s_mul_i32 s36, s35, 5\n"
  "v_add_u32 v12, s36, v0\n"
  "v_min_u32 v12, 143, v12\n"
  "v_lshlrev_b32 v12, 7, v12\n"
  "s_cmp_eq_u32 s3, 1\n"
  "s_cselect_b64 s[28:29], s[12:13], s[10:11]\n"
  "s_cmp_eq_u32 s3, 2\n"
  "s_cselect_b64 s[28:29], s[14:15], s[28:29]\n"
  "s_mov_b32 m0, 216\n"
  "s_mov_b64 exec, 31\n"
  "global_load_lds_dword v12, s[28:29]\n"
  "s_cmp_eq_u32 s3, 1\n"
  "s_cbranch_scc0 .Ldino_h1\n"
  "global_load_lds_dword v12, s[16:17]\n"
  ".Ldino_h1:\n"
  "s_mov_b64 exec, -1\n"
  "buffer_load_dword v19, off, s[24:27], 0 sc1\n"
  ".Ldino_nl1:\n"
  ".Ldino_pre:\n"
  "v_lshrrev_b32 v6, 4, v0\n"
  "v_and_b32 v7, 15, v0\n"
  "s_and_b32 s35, s2, 7\n"
  "s_bfe_u32 s36, s2, 0x10003\n"
  "s_bfe_u32 s37, s2, 0x30004\n"
  "s_lshl_b32 s28, s35, 1\n"
  "s_add_u32 s28, s28, s36\n"
  "s_lshl_b32 s35, s35, 10\n"
  "s_lshl_b32 s36, s36, 6\n"
  "s_lshl_b32 s29, s37, 7\n"
  "s_add_u32 s35, s35, s36\n"
  "s_add_u32 s35, s35, s29\n"
  "s_lshl_b32 s36, s3, 18\n"
  "s_add_u32 s35, s35, s36\n"
  "s_add_u32 s35, s35, 4096\n"
  "v_lshrrev_b32 v8, 2, v0\n"
  "v_and_b32 v9, 3, v0\n"
  "v_lshlrev_b32 v248, 13, v8\n"
  "v_lshl_add_u32 v248, v9, 4, v248\n"
  "v_add_u32 v248, s35, v248\n"
  "s_lshl_b32 s38, s3, 2\n"
  "v_mov_b32 v253, s38\n"
  "v_and_b32 v10, 1, v6\n"
  "v_lshrrev_b32 v11, 1, v6\n"
  "v_lshl_add_u32 v10, v10, 1, v11\n"
  "v_lshl_add_u32 v10, v10, 2, v9\n"
  "v_lshl_add_u32 v249, v10, 5, v253\n"
  "s_mul_i32 s37, s37, 12\n"
  "s_mul_i32 s36, s28, 96\n"
  "s_add_u32 s37, s37, s36\n"
  "v_lshl_add_u32 v252, v9, 2, v8\n"
  "v_add_lshl_u32 v252, v252, s37, 1\n"
  "s_lshl_b32 s28, s28, 2\n"
  "v_mov_b32 v250, s28\n"
  "s_mov_b32 s54, 131072\n"
  "s_mov_b32 s55, 0xfff\n"
  "s_mov_b32 s56, 0x07060302\n"
  "s_mov_b32 s57, 0x42480000\n"
  "s_mov_b32 s58, 0x88888888\n"
  "s_mov_b32 s59, 0x88888888\n"
  "s_cmp_eq_u32 s3, 3\n"
  "s_cbranch_scc1 .Ldino_ldone\n"
  "v_lshrrev_b32 v8, 2, v7\n"
  "v_mad_u32_u24 v9, v7, 12, v6\n"
  "v_lshlrev_b32 v10, 1, v8\n"
  "v_add_u32 v11, 4, v9\n"
  "v_xor_b32 v16, v9, v10\n"
  "v_xor_b32 v17, v11, v10\n"
  "v_lshlrev_b32 v16, 4, v16\n"
  "v_lshlrev_b32 v17, 4, v17\n"
  "s_cmp_lt_u32 s3, 3\n"
  "s_cbranch_scc1 .Ldino_helper\n"
  "s_add_u32 s22, s3, 1\n"
  "s_and_b32 s22, s22, 3\n"
  "s_mul_i32 s23, s22, 15360\n"
  "s_add_u32 s23, s23, 25088\n"
  "s_setprio 1\n"
  "v_lshl_add_u32 v19, v6, 2, v8\n"
  "v_and_b32 v20, 1, v7\n"
  "v_bfe_u32 v9, v7, 1, 1\n"
  "v_lshlrev_b32 v20, 3, v20\n"
  "v_add_u32 v16, s23, v16\n"
  "v_add_u32 v17, s23, v17\n"
  "v_add_u32 v20, s23, v20\n"
  "v_mad_u32_u24 v10, v19, 12, v9\n"
  "v_lshlrev_b32 v11, 5, v6\n"
  "v_lshlrev_b32 v10, 4, v10\n"
  "v_xad_u32 v21, v10, v11, v20\n"
  "v_add_u32 v10, 32, v10\n"
  "v_xad_u32 v22, v10, v11, v20\n"
  "v_add_u32 v10, 32, v10\n"
  "v_xad_u32 v23, v10, v11, v20\n"
  "v_add_u32 v10, 32, v10\n"
  "v_xad_u32 v24, v10, v11, v20\n"
  "v_add_u32 v10, 32, v10\n"
  "v_xad_u32 v25, v10, v11, v20\n"
  "v_add_u32 v10, 32, v10\n"
  "v_xad_u32 v26, v10, v11, v20\n"
  "v_and_b32 v10, 6, v7\n"
  "v_xor_b32 v10, v10, v6\n"
  "v_mul_u32_u24 v11, 384, v7\n"
  "v_lshl_add_u32 v27, v10, 4, v11\n"
  "v_add_u32 v27, s23, v27\n"
  "v_xor_b32 v28, 64, v27\n"
  "v_and_or_b32 v10, v19, 6, v9\n"
  "v_mul_u32_u24 v11, 384, v19\n"
  "v_lshl_add_u32 v10, v10, 4, v11\n"
  "v_add_u32 v29, v10, v20\n"
  "v_xor_b32 v30, 32, v29\n"
  "v_xor_b32 v31, 64, v29\n"
  "v_xor_b32 v32, 0x60, v29\n"
  "v_mov_b32 v36, 0x3f803f80\n"
  "s_mov_b32 s40, 0x11111111\n"
  "s_mov_b32 s41, 0x11111111\n"
  "v_cndmask_b32 v36, 0, v36, s[40:41]\n"
  "v_mov_b32 v37, v36\n"
  "v_mov_b32 v38, v36\n"
  "v_mov_b32 v39, v36\n"
  "s_mov_b64 s[40:41], 0xffff\n"
  "v_cndmask_b32 v42, 0, 1.0, s[40:41]\n"
  "s_lshl_b64 s[40:41], s[40:41], 32\n"
  "v_cndmask_b32 v43, 0, 1.0, s[40:41]\n"
  "s_mov_b32 s42, 0\n"
  "s_mov_b32 s43, -1\n"
  "s_mov_b32 s46, 0x00800080\n"
  "s_mov_b32 s47, 0x42c00000\n"
  "s_mov_b32 s48, 0x41a00000\n"
  "v_mov_b32 v41, 0x3dbcbe62\n"
  "s_lshl_b32 s38, s22, 2\n"
  "v_mov_b32 v33, s38\n"
  "s_barrier\n"
  ".Ldino_w_7:\n"
  "ds_read_b32 v8, v33 offset:80\n"
  "s_sleep 1\n"
  "s_waitcnt lgkmcnt(0)\n"
  "v_cmp_ne_u32 vcc, 1, v8\n"
  "s_cbranch_vccnz .Ldino_w_7\n"
  "ds_read_b128 v[164:167], v16 offset:0\n"
  "ds_read_b128 v[168:171], v17 offset:0\n"
  "ds_read_b128 v[172:175], v16 offset:128\n"
  "ds_read_b64_tr_b16 v[68:69], v21 offset:0\n"
  "ds_read_b64_tr_b16 v[70:71], v22 offset:0\n"
  "ds_read_b64_tr_b16 v[72:73], v23 offset:0\n"
  "ds_read_b64_tr_b16 v[74:75], v24 offset:0\n"
  "ds_read_b64_tr_b16 v[76:77], v25 offset:0\n"
  "ds_read_b64_tr_b16 v[78:79], v26 offset:0\n"
  "v_mov_b32 v40, 0\n"
  "s_waitcnt lgkmcnt(8)\n"
  "v_mfma_f32_16x16x32_bf16 v[44:47], v[36:39], v[164:167], 0\n"
  "v_add_u32 v56, s46, v164\n"
  "v_add_u32 v57, s46, v165\n"
  "v_add_u32 v58, s46, v166\n"
  "v_add_u32 v59, s46, v167\n"
  "s_waitcnt lgkmcnt(7)\n"
  "v_mfma_f32_16x16x32_bf16 v[44:47], v[36:39], v[168:171], v[44:47]\n"
  "v_add_u32 v60, s46, v168\n"
  "v_add_u32 v61, s46, v169\n"
  "v_add_u32 v62, s46, v170\n"
  "v_add_u32 v63, s46, v171\n"
  "s_waitcnt lgkmcnt(6)\n"
  "v_mfma_f32_16x16x32_bf16 v[44:47], v[36:39], v[172:175], v[44:47]\n"
  "v_add_u32 v64, s46, v172\n"
  "v_add_u32 v65, s46, v173\n"
  "v_add_u32 v66, s46, v174\n"
  "v_add_u32 v67, s46, v175\n"
  "v_dot2c_f32_bf16 v40, v164, v164\n"
  "v_dot2c_f32_bf16 v40, v165, v165\n"
  "v_dot2c_f32_bf16 v40, v166, v166\n"
  "v_dot2c_f32_bf16 v40, v167, v167\n"
  "v_dot2c_f32_bf16 v40, v168, v168\n"
  "v_dot2c_f32_bf16 v40, v169, v169\n"
  "v_dot2c_f32_bf16 v40, v170, v170\n"
  "v_dot2c_f32_bf16 v40, v171, v171\n"
  "v_dot2c_f32_bf16 v40, v172, v172\n"
  "v_dot2c_f32_bf16 v40, v173, v173\n"
  "v_dot2c_f32_bf16 v40, v174, v174\n"
  "v_dot2c_f32_bf16 v40, v175, v175\n"
  ".Ldino_w_9:\n"
  "ds_read_b32 v8, v33 offset:116\n"
  "s_sleep 1\n"
  "s_waitcnt lgkmcnt(0)\n"
  "v_cmp_ne_u32 vcc, 1, v8\n"
  "s_cbranch_vccnz .Ldino_w_9\n"
  "ds_read_b128 v[116:119], v27 offset:3072\n"
  "ds_read_b128 v[120:123], v28 offset:3072\n"
  "ds_read_b128 v[124:127], v27 offset:3200\n"
  "ds_read_b128 v[128:131], v28 offset:3200\n"
  "ds_read_b128 v[132:135], v27 offset:3328\n"
  "ds_read_b128 v[136:139], v28 offset:3328\n"
  "v_mov_b32 v184, v40\n"
  "v_mov_b32 v185, v40\n"
  "s_waitcnt lgkmcnt(5)\n"
  "v_mfma_f32_16x16x32_bf16 v[176:179], v[36:39], v[116:119], v[44:47]\n"
  "s_waitcnt lgkmcnt(4)\n"
  "v_mfma_f32_16x16x32_bf16 v[176:179], v[36:39], v[120:123], v[176:179]\n"
  "s_waitcnt lgkmcnt(3)\n"
  "v_mfma_f32_16x16x32_bf16 v[176:179], v[36:39], v[124:127], v[176:179]\n"
  "s_waitcnt lgkmcnt(2)\n"
  "v_mfma_f32_16x16x32_bf16 v[180:183], v[36:39], v[128:131], v[44:47]\n"
  "s_waitcnt lgkmcnt(1)\n"
  "v_mfma_f32_16x16x32_bf16 v[180:183], v[36:39], v[132:135], v[180:183]\n"
  "s_waitcnt lgkmcnt(0)\n"
  "v_mfma_f32_16x16x32_bf16 v[180:183], v[36:39], v[136:139], v[180:183]\n"
  "v_dot2c_f32_bf16 v184, v116, v116\n"
  "v_dot2c_f32_bf16 v184, v116, v56\n"
  "v_dot2c_f32_bf16 v185, v128, v128\n"
  "v_dot2c_f32_bf16 v185, v128, v56\n"
  "v_dot2c_f32_bf16 v184, v117, v117\n"
  "v_dot2c_f32_bf16 v184, v117, v57\n"
  "v_dot2c_f32_bf16 v185, v129, v129\n"
  "v_dot2c_f32_bf16 v185, v129, v57\n"
  "v_dot2c_f32_bf16 v184, v118, v118\n"
  "v_dot2c_f32_bf16 v184, v118, v58\n"
  "v_dot2c_f32_bf16 v185, v130, v130\n"
  "v_dot2c_f32_bf16 v185, v130, v58\n"
  "v_dot2c_f32_bf16 v184, v119, v119\n"
  "v_dot2c_f32_bf16 v184, v119, v59\n"
  "v_dot2c_f32_bf16 v185, v131, v131\n"
  "v_dot2c_f32_bf16 v185, v131, v59\n"
  "v_dot2c_f32_bf16 v184, v120, v120\n"
  "v_dot2c_f32_bf16 v184, v120, v60\n"
  "v_dot2c_f32_bf16 v185, v132, v132\n"
  "v_dot2c_f32_bf16 v185, v132, v60\n"
  "v_dot2c_f32_bf16 v184, v121, v121\n"
  "v_dot2c_f32_bf16 v184, v121, v61\n"
  "v_dot2c_f32_bf16 v185, v133, v133\n"
  "v_dot2c_f32_bf16 v185, v133, v61\n"
  "v_dot2c_f32_bf16 v184, v122, v122\n"
  "v_dot2c_f32_bf16 v184, v122, v62\n"
  "v_dot2c_f32_bf16 v185, v134, v134\n"
  "v_dot2c_f32_bf16 v185, v134, v62\n"
  "v_dot2c_f32_bf16 v184, v123, v123\n"
  "v_dot2c_f32_bf16 v184, v123, v63\n"
  "v_dot2c_f32_bf16 v185, v135, v135\n"
  "v_dot2c_f32_bf16 v185, v135, v63\n"
  "v_dot2c_f32_bf16 v184, v124, v124\n"
  "v_dot2c_f32_bf16 v184, v124, v64\n"
  "v_dot2c_f32_bf16 v185, v136, v136\n"
  "v_dot2c_f32_bf16 v185, v136, v64\n"
  "v_dot2c_f32_bf16 v184, v125, v125\n"
  "v_dot2c_f32_bf16 v184, v125, v65\n"
  "v_dot2c_f32_bf16 v185, v137, v137\n"
  "v_dot2c_f32_bf16 v185, v137, v65\n"
  "v_dot2c_f32_bf16 v184, v126, v126\n"
  "v_dot2c_f32_bf16 v184, v126, v66\n"
  "v_dot2c_f32_bf16 v185, v138, v138\n"
  "v_dot2c_f32_bf16 v185, v138, v66\n"
  "v_dot2c_f32_bf16 v184, v127, v127\n"
  "v_dot2c_f32_bf16 v184, v127, v67\n"
  "v_dot2c_f32_bf16 v185, v139, v139\n"
  "v_dot2c_f32_bf16 v185, v139, v67\n"
  "v_cndmask_b32 v176, v176, v180, s[42:43]\n"
  "s_nop 1\n"
  "v_permlane32_swap_b32 v184, v185\n"
  "v_add_f32 v184, v184, v185\n"
  "v_mov_b32 v185, v184\n"
  "s_nop 1\n"
  "v_permlane16_swap_b32 v184, v185\n"
  "v_add_f32 v184, v184, v185\n"
  "v_fma_f32 v184, v184, s47, v41\n"
  "v_fma_f32 v184, -v176, v176, v184\n"
  "v_max_f32 v184, v184, v41\n"
  "v_rsq_f32 v184, v184\n"
  "s_nop 1\n"
  "v_mfma_f32_16x16x4_f32 v[204:207], v184, v42, 0\n"
  "v_mfma_f32_16x16x4_f32 v[208:211], v184, v43, 0\n"
  ".Ldino_w_11:\n"
  "ds_read_b32 v8, v33 offset:152\n"
  "s_sleep 1\n"
  "s_waitcnt lgkmcnt(0)\n"
  "v_cmp_ne_u32 vcc, 1, v8\n"
  "s_cbranch_vccnz .Ldino_w_11\n"
  "ds_read_b128 v[140:143], v27 offset:9216\n"
  "ds_read_b128 v[144:147], v28 offset:9216\n"
  "ds_read_b128 v[148:151], v27 offset:9344\n"
  "ds_read_b128 v[152:155], v28 offset:9344\n"
  "ds_read_b128 v[156:159], v27 offset:9472\n"
  "ds_read_b128 v[160:163], v28 offset:9472\n"
  "v_mov_b32 v184, v40\n"
  "v_mov_b32 v185, v40\n"
  "s_waitcnt lgkmcnt(5)\n"
  "v_mfma_f32_16x16x32_bf16 v[176:179], v[36:39], v[140:143], v[44:47]\n"
  "s_waitcnt lgkmcnt(4)\n"
  "v_mfma_f32_16x16x32_bf16 v[176:179], v[36:39], v[144:147], v[176:179]\n"
  "s_waitcnt lgkmcnt(3)\n"
  "v_mfma_f32_16x16x32_bf16 v[176:179], v[36:39], v[148:151], v[176:179]\n"
  "s_waitcnt lgkmcnt(2)\n"
  "v_mfma_f32_16x16x32_bf16 v[180:183], v[36:39], v[152:155], v[44:47]\n"
  "s_waitcnt lgkmcnt(1)\n"
  "v_mfma_f32_16x16x32_bf16 v[180:183], v[36:39], v[156:159], v[180:183]\n"
  "s_waitcnt lgkmcnt(0)\n"
  "v_mfma_f32_16x16x32_bf16 v[180:183], v[36:39], v[160:163], v[180:183]\n"
  "v_dot2c_f32_bf16 v184, v140, v140\n"
  "v_dot2c_f32_bf16 v184, v140, v56\n"
  "v_dot2c_f32_bf16 v185, v152, v152\n"
  "v_dot2c_f32_bf16 v185, v152, v56\n"
  "v_dot2c_f32_bf16 v184, v141, v141\n"
  "v_dot2c_f32_bf16 v184, v141, v57\n"
  "v_dot2c_f32_bf16 v185, v153, v153\n"
  "v_dot2c_f32_bf16 v185, v153, v57\n"
  "v_dot2c_f32_bf16 v184, v142, v142\n"
  "v_dot2c_f32_bf16 v184, v142, v58\n"
  "v_dot2c_f32_bf16 v185, v154, v154\n"
  "v_dot2c_f32_bf16 v185, v154, v58\n"
  "v_dot2c_f32_bf16 v184, v143, v143\n"
  "v_dot2c_f32_bf16 v184, v143, v59\n"
  "v_dot2c_f32_bf16 v185, v155, v155\n"
  "v_dot2c_f32_bf16 v185, v155, v59\n"
  "v_dot2c_f32_bf16 v184, v144, v144\n"
  "v_dot2c_f32_bf16 v184, v144, v60\n"
  "v_dot2c_f32_bf16 v185, v156, v156\n"
  "v_dot2c_f32_bf16 v185, v156, v60\n"
  "v_dot2c_f32_bf16 v184, v145, v145\n"
  "v_dot2c_f32_bf16 v184, v145, v61\n"
  "v_dot2c_f32_bf16 v185, v157, v157\n"
  "v_dot2c_f32_bf16 v185, v157, v61\n"
  "v_dot2c_f32_bf16 v184, v146, v146\n"
  "v_dot2c_f32_bf16 v184, v146, v62\n"
  "v_dot2c_f32_bf16 v185, v158, v158\n"
  "v_dot2c_f32_bf16 v185, v158, v62\n"
  "v_dot2c_f32_bf16 v184, v147, v147\n"
  "v_dot2c_f32_bf16 v184, v147, v63\n"
  "v_dot2c_f32_bf16 v185, v159, v159\n"
  "v_dot2c_f32_bf16 v185, v159, v63\n"
  "v_dot2c_f32_bf16 v184, v148, v148\n"
  "v_dot2c_f32_bf16 v184, v148, v64\n"
  "v_dot2c_f32_bf16 v185, v160, v160\n"
  "v_dot2c_f32_bf16 v185, v160, v64\n"
  "v_dot2c_f32_bf16 v184, v149, v149\n"
  "v_dot2c_f32_bf16 v184, v149, v65\n"
  "v_dot2c_f32_bf16 v185, v161, v161\n"
  "v_dot2c_f32_bf16 v185, v161, v65\n"
  "v_dot2c_f32_bf16 v184, v150, v150\n"
  "v_dot2c_f32_bf16 v184, v150, v66\n"
  "v_dot2c_f32_bf16 v185, v162, v162\n"
  "v_dot2c_f32_bf16 v185, v162, v66\n"
  "v_dot2c_f32_bf16 v184, v151, v151\n"
  "v_dot2c_f32_bf16 v184, v151, v67\n"
  "v_dot2c_f32_bf16 v185, v163, v163\n"
  "v_dot2c_f32_bf16 v185, v163, v67\n"
  "v_cndmask_b32 v176, v176, v180, s[42:43]\n"
  "s_nop 1\n"
  "v_permlane32_swap_b32 v184, v185\n"
  "v_add_f32 v184, v184, v185\n"
  "v_mov_b32 v185, v184\n"
  "s_nop 1\n"
  "v_permlane16_swap_b32 v184, v185\n"
  "v_add_f32 v184, v184, v185\n"
  "v_fma_f32 v184, v184, s47, v41\n"
  "v_fma_f32 v184, -v176, v176, v184\n"
  "v_max_f32 v184, v184, v41\n"
  "v_rsq_f32 v184, v184\n"
  "s_nop 1\n"
  "v_mfma_f32_16x16x4_f32 v[212:215], v184, v42, 0\n"
  "v_mfma_f32_16x16x4_f32 v[216:219], v184, v43, 0\n"
  ".Ldino_w_13:\n"
  "ds_read_b32 v8, v4 offset:8\n"
  "s_sleep 1\n"
  "s_waitcnt lgkmcnt(0)\n"
  "v_cmp_ne_u32 vcc, 0x10101, v8\n"
  "s_cbranch_vccnz .Ldino_w_13\n"
  "ds_read_b128 v[80:83], v18 offset:3584\n"
  "ds_read_b128 v[84:87], v18 offset:4608\n"
  "ds_read_b128 v[88:91], v18 offset:5632\n"
  "ds_read_b32 v8, v4 offset:192\n"
  "s_waitcnt lgkmcnt(3)\n"
  "v_mfma_f32_16x16x32_bf16 v[48:51], v[164:167], v[80:83], 0\n"
  "s_waitcnt lgkmcnt(2)\n"
  "v_mfma_f32_16x16x32_bf16 v[48:51], v[168:171], v[84:87], v[48:51]\n"
  "s_waitcnt lgkmcnt(1)\n"
  "v_mfma_f32_16x16x32_bf16 v[48:51], v[172:175], v[88:91], v[48:51]\n"
  "s_waitcnt lgkmcnt(0)\n"
  "v_readfirstlane_b32 s53, v8\n"
  "v_mfma_f32_16x16x32_bf16 v[188:191], v[116:119], v[80:83], v[48:51]\n"
  "v_mfma_f32_16x16x32_bf16 v[188:191], v[120:123], v[84:87], v[188:191]\n"
  "v_mfma_f32_16x16x32_bf16 v[188:191], v[124:127], v[88:91], v[188:191]\n"
  "v_mfma_f32_16x16x32_bf16 v[192:195], v[128:131], v[80:83], v[48:51]\n"
  "v_mfma_f32_16x16x32_bf16 v[192:195], v[132:135], v[84:87], v[192:195]\n"
  "v_mfma_f32_16x16x32_bf16 v[192:195], v[136:139], v[88:91], v[192:195]\n"
  "v_mfma_f32_16x16x32_bf16 v[196:199], v[140:143], v[80:83], v[48:51]\n"
  "ds_read_b64_tr_b16 v[116:117], v29 offset:3072\n"
  "ds_read_b64_tr_b16 v[118:119], v31 offset:3200\n"
  "v_mfma_f32_16x16x32_bf16 v[196:199], v[144:147], v[84:87], v[196:199]\n"
  "ds_read_b64_tr_b16 v[120:121], v30 offset:3072\n"
  "ds_read_b64_tr_b16 v[122:123], v32 offset:3200\n"
  "v_mfma_f32_16x16x32_bf16 v[196:199], v[148:151], v[88:91], v[196:199]\n"
  "ds_read_b64_tr_b16 v[124:125], v31 offset:3072\n"
  "ds_read_b64_tr_b16 v[126:127], v29 offset:3328\n"
  "v_mfma_f32_16x16x32_bf16 v[200:203], v[152:155], v[80:83], v[48:51]\n"
  "ds_read_b64_tr_b16 v[128:129], v32 offset:3072\n"
  "ds_read_b64_tr_b16 v[130:131], v30 offset:3328\n"
  "v_mfma_f32_16x16x32_bf16 v[200:203], v[156:159], v[84:87], v[200:203]\n"
  "ds_read_b64_tr_b16 v[132:133], v29 offset:3200\n"
  "ds_read_b64_tr_b16 v[134:135], v31 offset:3328\n"
  "v_mfma_f32_16x16x32_bf16 v[200:203], v[160:163], v[88:91], v[200:203]\n"
  "ds_read_b64_tr_b16 v[136:137], v30 offset:3200\n"
  "ds_read_b64_tr_b16 v[138:139], v32 offset:3328\n"
  "v_pk_mul_f32 v[188:189], v[188:189], v[204:205]\n"
  "v_pk_mul_f32 v[190:191], v[190:191], v[206:207]\n"
  "v_pk_mul_f32 v[192:193], v[192:193], v[208:209]\n"
  "v_pk_mul_f32 v[194:195], v[194:195], v[210:211]\n"
  "ds_read_b64_tr_b16 v[140:141], v29 offset:9216\n"
  "ds_read_b64_tr_b16 v[142:143], v31 offset:9344\n"
  "ds_read_b64_tr_b16 v[144:145], v30 offset:9216\n"
  "ds_read_b64_tr_b16 v[146:147], v32 offset:9344\n"
  "ds_read_b64_tr_b16 v[148:149], v31 offset:9216\n"
  "ds_read_b64_tr_b16 v[150:151], v29 offset:9472\n"
  "ds_read_b64_tr_b16 v[152:153], v32 offset:9216\n"
  "ds_read_b64_tr_b16 v[154:155], v30 offset:9472\n"
  "ds_read_b64_tr_b16 v[156:157], v29 offset:9344\n"
  "ds_read_b64_tr_b16 v[158:159], v31 offset:9472\n"
  "ds_read_b64_tr_b16 v[160:161], v30 offset:9344\n"
  "ds_read_b64_tr_b16 v[162:163], v32 offset:9472\n"
  "v_pk_mul_f32 v[196:197], v[196:197], v[212:213]\n"
  "v_pk_mul_f32 v[198:199], v[198:199], v[214:215]\n"
  "v_pk_mul_f32 v[200:201], v[200:201], v[216:217]\n"
  "v_pk_mul_f32 v[202:203], v[202:203], v[218:219]\n"
  "v_max3_f32 v34, v188, v189, v190\n"
  "v_max3_f32 v244, v191, v192, v193\n"
  "v_max3_f32 v34, v34, v194, v195\n"
  "v_max3_f32 v244, v244, v196, v197\n"
  "v_max3_f32 v34, v34, v198, v199\n"
  "v_max3_f32 v244, v244, v200, v201\n"
  "v_max3_f32 v34, v34, v202, v203\n"
  "v_max_f32 v34, v34, v244\n"
  "v_mov_b32 v244, v34\n"
  "s_nop 1\n"
  "v_permlane16_swap_b32 v34, v244\n"
  "v_max_f32 v34, v34, v244\n"
  "v_mov_b32 v244, v34\n"
  "s_nop 1\n"
  "v_permlane32_swap_b32 v34, v244\n"
  "v_max_f32 v34, v34, v244\n"
  "v_pk_add_f32 v[188:189], v[188:189], v[34:35] op_sel_hi:[1,0] neg_lo:[0,1] neg_hi:[0,1]\n"
  "v_pk_add_f32 v[190:191], v[190:191], v[34:35] op_sel_hi:[1,0] neg_lo:[0,1] neg_hi:[0,1]\n"
  "v_pk_add_f32 v[192:193], v[192:193], v[34:35] op_sel_hi:[1,0] neg_lo:[0,1] neg_hi:[0,1]\n"
  "v_pk_add_f32 v[194:195], v[194:195], v[34:35] op_sel_hi:[1,0] neg_lo:[0,1] neg_hi:[0,1]\n"
  "v_pk_add_f32 v[196:197], v[196:197], v[34:35] op_sel_hi:[1,0] neg_lo:[0,1] neg_hi:[0,1]\n"
  "v_pk_add_f32 v[198:199], v[198:199], v[34:35] op_sel_hi:[1,0] neg_lo:[0,1] neg_hi:[0,1]\n"
  "v_pk_add_f32 v[200:201], v[200:201], v[34:35] op_sel_hi:[1,0] neg_lo:[0,1] neg_hi:[0,1]\n"
  "v_pk_add_f32 v[202:203], v[202:203], v[34:35] op_sel_hi:[1,0] neg_lo:[0,1] neg_hi:[0,1]\n"
  "v_exp_f32 v220, v188\n"
  "v_exp_f32 v221, v189\n"
  "v_exp_f32 v222, v190\n"
  "v_exp_f32 v223, v191\n"
  "v_exp_f32 v224, v192\n"
  "v_exp_f32 v225, v193\n"
  "v_exp_f32 v226, v194\n"
  "v_exp_f32 v227, v195\n"
  "v_pk_mul_f32 v[188:189], v[220:221], v[204:205]\n"
  "v_pk_mul_f32 v[190:191], v[222:223], v[206:207]\n"
  "v_pk_mul_f32 v[192:193], v[224:225], v[208:209]\n"
  "v_pk_mul_f32 v[194:195], v[226:227], v[210:211]\n"
  "v_cvt_pk_bf16_f32 v236, v188, v189\n"
  "v_cvt_pk_bf16_f32 v237, v190, v191\n"
  "v_cvt_pk_bf16_f32 v238, v192, v193\n"
  "v_cvt_pk_bf16_f32 v239, v194, v195\n"
  "v_exp_f32 v228, v196\n"
  "s_waitcnt lgkmcnt(0)\n"
  "v_mfma_f32_16x16x32_bf16 v[92:95], v[116:119], v[236:239], 0\n"
  "v_exp_f32 v229, v197\n"
  "v_mfma_f32_16x16x32_bf16 v[96:99], v[120:123], v[236:239], 0\n"
  "v_exp_f32 v230, v198\n"
  "v_mfma_f32_16x16x32_bf16 v[100:103], v[124:127], v[236:239], 0\n"
  "v_exp_f32 v231, v199\n"
  "v_mfma_f32_16x16x32_bf16 v[104:107], v[128:131], v[236:239], 0\n"
  "v_exp_f32 v232, v200\n"
  "v_mfma_f32_16x16x32_bf16 v[108:111], v[132:135], v[236:239], 0\n"
  "v_exp_f32 v233, v201\n"
  "v_mfma_f32_16x16x32_bf16 v[112:115], v[136:139], v[236:239], 0\n"
  "v_exp_f32 v234, v202\n"
  "v_exp_f32 v235, v203\n"
  "v_pk_mul_f32 v[196:197], v[228:229], v[212:213]\n"
  "v_pk_mul_f32 v[198:199], v[230:231], v[214:215]\n"
  "v_pk_mul_f32 v[200:201], v[232:233], v[216:217]\n"
  "v_pk_mul_f32 v[202:203], v[234:235], v[218:219]\n"
  "v_cvt_pk_bf16_f32 v240, v196, v197\n"
  "v_cvt_pk_bf16_f32 v241, v198, v199\n"
  "v_cvt_pk_bf16_f32 v242, v200, v201\n"
  "v_cvt_pk_bf16_f32 v243, v202, v203\n"
  "v_pk_add_f32 v[188:189], v[188:189], v[192:193]\n"
  "v_pk_add_f32 v[190:191], v[190:191], v[194:195]\n"
  "v_mfma_f32_16x16x32_bf16 v[92:95], v[140:143], v[240:243], v[92:95]\n"
  "v_pk_add_f32 v[196:197], v[196:197], v[200:201]\n"
  "v_pk_add_f32 v[198:199], v[198:199], v[202:203]\n"
  "v_mfma_f32_16x16x32_bf16 v[96:99], v[144:147], v[240:243], v[96:99]\n"
  "v_pk_add_f32 v[188:189], v[188:189], v[196:197]\n"
  "v_pk_add_f32 v[190:191], v[190:191], v[198:199]\n"
  "v_mfma_f32_16x16x32_bf16 v[100:103], v[148:151], v[240:243], v[100:103]\n"
  "v_cvt_pk_bf16_f32 v52, v188, v189\n"
  "v_cvt_pk_bf16_f32 v53, v190, v191\n"
  "v_mfma_f32_16x16x32_bf16 v[104:107], v[152:155], v[240:243], v[104:107]\n"
  "v_pk_add_f32 v[220:221], v[220:221], v[222:223]\n"
  "v_pk_add_f32 v[224:225], v[224:225], v[226:227]\n"
  "v_mfma_f32_16x16x32_bf16 v[108:111], v[156:159], v[240:243], v[108:111]\n"
  "v_pk_add_f32 v[228:229], v[228:229], v[230:231]\n"
  "v_pk_add_f32 v[232:233], v[232:233], v[234:235]\n"
  "v_mfma_f32_16x16x32_bf16 v[112:115], v[160:163], v[240:243], v[112:115]\n"
  "v_pk_add_f32 v[220:221], v[220:221], v[224:225]\n"
  "v_pk_add_f32 v[228:229], v[228:229], v[232:233]\n"
  "v_pk_add_f32 v[220:221], v[220:221], v[228:229]\n"
  "v_add_f32 v35, v220, v221\n"
  ".Ldino_next:\n"
  "v_mfma_f32_16x16x16_bf16 v[92:95], v[68:69], v[52:53], v[92:95]\n"
  "v_add_u32 v33, 16, v33\n"
  "s_add_u32 s22, s22, 4\n"
  "s_add_u32 s23, s23, 61440\n"
  "v_mfma_f32_16x16x16_bf16 v[96:99], v[70:71], v[52:53], v[96:99]\n"
  "v_add_u32 v16, 61440, v16\n"
  "v_add_u32 v17, 61440, v17\n"
  "v_add_u32 v21, 61440, v21\n"
  "v_mfma_f32_16x16x16_bf16 v[100:103], v[72:73], v[52:53], v[100:103]\n"
  "v_add_u32 v22, 61440, v22\n"
  "v_add_u32 v23, 61440, v23\n"
  "v_add_u32 v24, 61440, v24\n"
  "v_mfma_f32_16x16x16_bf16 v[104:107], v[74:75], v[52:53], v[104:107]\n"
  "v_add_u32 v25, 61440, v25\n"
  "v_add_u32 v26, 61440, v26\n"
  "v_add_u32 v27, 61440, v27\n"
  "v_mfma_f32_16x16x16_bf16 v[108:111], v[76:77], v[52:53], v[108:111]\n"
  "v_add_u32 v28, 61440, v28\n"
  "v_add_u32 v29, 61440, v29\n"
  "v_add_u32 v30, 61440, v30\n"
  "v_mfma_f32_16x16x16_bf16 v[112:115], v[78:79], v[52:53], v[112:115]\n"
  "v_add_u32 v31, 61440, v31\n"
  "v_add_u32 v32, 61440, v32\n"
  ".Ldino_w_15:\n"
  "ds_read_b32 v8, v33 offset:80\n"
  "s_sleep 1\n"
  "s_waitcnt lgkmcnt(0)\n"
  "v_cmp_ne_u32 vcc, 1, v8\n"
  "s_cbranch_vccnz .Ldino_w_15\n"
  "ds_read_b128 v[164:167], v16 offset:0\n"
  "ds_read_b128 v[168:171], v17 offset:0\n"
  "ds_read_b128 v[172:175], v16 offset:128\n"
  "ds_read_b64_tr_b16 v[68:69], v21 offset:0\n"
  "ds_read_b64_tr_b16 v[70:71], v22 offset:0\n"
  "ds_read_b64_tr_b16 v[72:73], v23 offset:0\n"
  "ds_read_b64_tr_b16 v[74:75], v24 offset:0\n"
  "ds_read_b64_tr_b16 v[76:77], v25 offset:0\n"
  "ds_read_b64_tr_b16 v[78:79], v26 offset:0\n"
  "v_mov_b32 v40, 0\n"
  "s_waitcnt lgkmcnt(8)\n"
  "v_mfma_f32_16x16x32_bf16 v[44:47], v[36:39], v[164:167], 0\n"
  "v_add_u32 v56, s46, v164\n"
  "v_add_u32 v57, s46, v165\n"
  "v_mfma_f32_16x16x32_bf16 v[48:51], v[164:167], v[80:83], 0\n"
  "v_add_u32 v58, s46, v166\n"
  "v_add_u32 v59, s46, v167\n"
  "s_waitcnt lgkmcnt(7)\n"
  "v_mfma_f32_16x16x32_bf16 v[44:47], v[36:39], v[168:171], v[44:47]\n"
  "v_add_u32 v60, s46, v168\n"
  "v_add_u32 v61, s46, v169\n"
  "v_mfma_f32_16x16x32_bf16 v[48:51], v[168:171], v[84:87], v[48:51]\n"
  "v_add_u32 v62, s46, v170\n"
  "v_add_u32 v63, s46, v171\n"
  "s_waitcnt lgkmcnt(6)\n"
  "v_mfma_f32_16x16x32_bf16 v[44:47], v[36:39], v[172:175], v[44:47]\n"
  "v_add_u32 v64, s46, v172\n"
  "v_add_u32 v65, s46, v173\n"
  "v_mfma_f32_16x16x32_bf16 v[48:51], v[172:175], v[88:91], v[48:51]\n"
  "v_add_u32 v66, s46, v174\n"
  "v_add_u32 v67, s46, v175\n"
  "v_dot2c_f32_bf16 v40, v164, v164\n"
  "v_dot2c_f32_bf16 v40, v165, v165\n"
  "v_dot2c_f32_bf16 v40, v166, v166\n"
  "v_dot2c_f32_bf16 v40, v167, v167\n"
  "v_dot2c_f32_bf16 v40, v168, v168\n"
  "v_dot2c_f32_bf16 v40, v169, v169\n"
  "v_dot2c_f32_bf16 v40, v170, v170\n"
  "v_dot2c_f32_bf16 v40, v171, v171\n"
  "v_dot2c_f32_bf16 v40, v172, v172\n"
  "v_dot2c_f32_bf16 v40, v173, v173\n"
  "v_dot2c_f32_bf16 v40, v174, v174\n"
  "v_dot2c_f32_bf16 v40, v175, v175\n"
  ".Ldino_w_17:\n"
  "ds_read_b32 v8, v33 offset:116\n"
  "s_sleep 1\n"
  "s_waitcnt lgkmcnt(0)\n"
  "v_cmp_ne_u32 vcc, 1, v8\n"
  "s_cbranch_vccnz .Ldino_w_17\n"
  "ds_read_b128 v[116:119], v27 offset:3072\n"
  "ds_read_b128 v[120:123], v28 offset:3072\n"
  "ds_read_b128 v[124:127], v27 offset:3200\n"
  "ds_read_b128 v[128:131], v28 offset:3200\n"
  "ds_read_b128 v[132:135], v27 offset:3328\n"
  "ds_read_b128 v[136:139], v28 offset:3328\n"
  "v_mov_b32 v184, v40\n"
  "v_mov_b32 v185, v40\n"
  "s_waitcnt lgkmcnt(5)\n"
  "v_mfma_f32_16x16x32_bf16 v[176:179], v[36:39], v[116:119], v[44:47]\n"
  "s_waitcnt lgkmcnt(4)\n"
  "v_mfma_f32_16x16x32_bf16 v[176:179], v[36:39], v[120:123], v[176:179]\n"
  "s_waitcnt lgkmcnt(3)\n"
  "v_mfma_f32_16x16x32_bf16 v[176:179], v[36:39], v[124:127], v[176:179]\n"
  "s_waitcnt lgkmcnt(2)\n"
  "v_mfma_f32_16x16x32_bf16 v[180:183], v[36:39], v[128:131], v[44:47]\n"
  "s_waitcnt lgkmcnt(1)\n"
  "v_mfma_f32_16x16x32_bf16 v[180:183], v[36:39], v[132:135], v[180:183]\n"
  "s_waitcnt lgkmcnt(0)\n"
  "v_mfma_f32_16x16x32_bf16 v[180:183], v[36:39], v[136:139], v[180:183]\n"
  "v_dot2c_f32_bf16 v184, v116, v116\n"
  "v_dot2c_f32_bf16 v184, v116, v56\n"
  "v_dot2c_f32_bf16 v185, v128, v128\n"
  "v_dot2c_f32_bf16 v185, v128, v56\n"
  "v_dot2c_f32_bf16 v184, v117, v117\n"
  "v_dot2c_f32_bf16 v184, v117, v57\n"
  "v_dot2c_f32_bf16 v185, v129, v129\n"
  "v_dot2c_f32_bf16 v185, v129, v57\n"
  "v_dot2c_f32_bf16 v184, v118, v118\n"
  "v_dot2c_f32_bf16 v184, v118, v58\n"
  "v_dot2c_f32_bf16 v185, v130, v130\n"
  "v_dot2c_f32_bf16 v185, v130, v58\n"
  "v_dot2c_f32_bf16 v184, v119, v119\n"
  "v_dot2c_f32_bf16 v184, v119, v59\n"
  "v_dot2c_f32_bf16 v185, v131, v131\n"
  "v_dot2c_f32_bf16 v185, v131, v59\n"
  "v_dot2c_f32_bf16 v184, v120, v120\n"
  "v_dot2c_f32_bf16 v184, v120, v60\n"
  "v_dot2c_f32_bf16 v185, v132, v132\n"
  "v_dot2c_f32_bf16 v185, v132, v60\n"
  "v_dot2c_f32_bf16 v184, v121, v121\n"
  "v_dot2c_f32_bf16 v184, v121, v61\n"
  "v_dot2c_f32_bf16 v185, v133, v133\n"
  "v_dot2c_f32_bf16 v185, v133, v61\n"
  "v_dot2c_f32_bf16 v184, v122, v122\n"
  "v_dot2c_f32_bf16 v184, v122, v62\n"
  "v_dot2c_f32_bf16 v185, v134, v134\n"
  "v_dot2c_f32_bf16 v185, v134, v62\n"
  "v_dot2c_f32_bf16 v184, v123, v123\n"
  "v_dot2c_f32_bf16 v184, v123, v63\n"
  "v_dot2c_f32_bf16 v185, v135, v135\n"
  "v_dot2c_f32_bf16 v185, v135, v63\n"
  "v_dot2c_f32_bf16 v184, v124, v124\n"
  "v_dot2c_f32_bf16 v184, v124, v64\n"
  "v_dot2c_f32_bf16 v185, v136, v136\n"
  "v_dot2c_f32_bf16 v185, v136, v64\n"
  "v_dot2c_f32_bf16 v184, v125, v125\n"
  "v_dot2c_f32_bf16 v184, v125, v65\n"
  "v_dot2c_f32_bf16 v185, v137, v137\n"
  "v_dot2c_f32_bf16 v185, v137, v65\n"
  "v_dot2c_f32_bf16 v184, v126, v126\n"
  "v_dot2c_f32_bf16 v184, v126, v66\n"
  "v_dot2c_f32_bf16 v185, v138, v138\n"
  "v_dot2c_f32_bf16 v185, v138, v66\n"
  "v_dot2c_f32_bf16 v184, v127, v127\n"
  "v_dot2c_f32_bf16 v184, v127, v67\n"
  "v_dot2c_f32_bf16 v185, v139, v139\n"
  "v_dot2c_f32_bf16 v185, v139, v67\n"
  "v_mfma_f32_16x16x32_bf16 v[188:191], v[116:119], v[80:83], v[48:51]\n"
  "v_cndmask_b32 v176, v176, v180, s[42:43]\n"
  "v_mfma_f32_16x16x32_bf16 v[188:191], v[120:123], v[84:87], v[188:191]\n"
  "v_permlane32_swap_b32 v184, v185\n"
  "v_add_f32 v184, v184, v185\n"
  "v_mfma_f32_16x16x32_bf16 v[188:191], v[124:127], v[88:91], v[188:191]\n"
  "v_mov_b32 v185, v184\n"
  "v_mfma_f32_16x16x32_bf16 v[192:195], v[128:131], v[80:83], v[48:51]\n"
  "s_nop 0\n"
  "v_permlane16_swap_b32 v184, v185\n"
  "v_add_f32 v184, v184, v185\n"
  "v_mfma_f32_16x16x32_bf16 v[192:195], v[132:135], v[84:87], v[192:195]\n"
  "v_fma_f32 v184, v184, s47, v41\n"
  "v_fma_f32 v184, -v176, v176, v184\n"
  "v_mfma_f32_16x16x32_bf16 v[192:195], v[136:139], v[88:91], v[192:195]\n"
  "v_max_f32 v184, v184, v41\n"
  "v_rsq_f32 v184, v184\n"
  "ds_read_b64_tr_b16 v[116:117], v29 offset:3072\n"
  "ds_read_b64_tr_b16 v[118:119], v31 offset:3200\n"
  "v_mfma_f32_16x16x4_f32 v[204:207], v184, v42, 0\n"
  "ds_read_b64_tr_b16 v[120:121], v30 offset:3072\n"
  "ds_read_b64_tr_b16 v[122:123], v32 offset:3200\n"
  "ds_read_b64_tr_b16 v[124:125], v31 offset:3072\n"
  "ds_read_b64_tr_b16 v[126:127], v29 offset:3328\n"
  "v_mfma_f32_16x16x4_f32 v[208:211], v184, v43, 0\n"
  "ds_read_b64_tr_b16 v[128:129], v32 offset:3072\n"
  "ds_read_b64_tr_b16 v[130:131], v30 offset:3328\n"
  "ds_read_b64_tr_b16 v[132:133], v29 offset:3200\n"
  "ds_read_b64_tr_b16 v[134:135], v31 offset:3328\n"
  "ds_read_b64_tr_b16 v[136:137], v30 offset:3200\n"
  "ds_read_b64_tr_b16 v[138:139], v32 offset:3328\n"
  "v_pk_fma_f32 v[188:189], v[188:189], v[204:205], v[34:35] op_sel_hi:[1,1,0] neg_lo:[0,0,1] neg_hi:[0,0,1]\n"
  "v_pk_fma_f32 v[190:191], v[190:191], v[206:207], v[34:35] op_sel_hi:[1,1,0] neg_lo:[0,0,1] neg_hi:[0,0,1]\n"
  "s_nop 1\n"
  "v_pk_fma_f32 v[192:193], v[192:193], v[208:209], v[34:35] op_sel_hi:[1,1,0] neg_lo:[0,0,1] neg_hi:[0,0,1]\n"
  "v_pk_fma_f32 v[194:195], v[194:195], v[210:211], v[34:35] op_sel_hi:[1,1,0] neg_lo:[0,0,1] neg_hi:[0,0,1]\n"
  "v_max3_f32 v244, v188, v189, v190\n"
  "v_max3_f32 v245, v191, v192, v193\n"
  "v_max3_f32 v244, v244, v194, v195\n"
  "v_max_f32 v244, v244, v245\n"
  "v_cmp_lt_f32 vcc, s48, v244\n"
  ".Ldino_redo0:\n"
  "v_exp_f32 v220, v188\n"
  "v_exp_f32 v221, v189\n"
  "v_exp_f32 v222, v190\n"
  "v_exp_f32 v223, v191\n"
  "v_exp_f32 v224, v192\n"
  "v_exp_f32 v225, v193\n"
  "v_exp_f32 v226, v194\n"
  "v_exp_f32 v227, v195\n"
  "s_cbranch_vccnz .Ldino_rare0\n"
  "v_pk_mul_f32 v[188:189], v[220:221], v[204:205]\n"
  "v_pk_mul_f32 v[190:191], v[222:223], v[206:207]\n"
  "v_pk_mul_f32 v[192:193], v[224:225], v[208:209]\n"
  "v_pk_mul_f32 v[194:195], v[226:227], v[210:211]\n"
  "v_cvt_pk_bf16_f32 v236, v188, v189\n"
  "v_cvt_pk_bf16_f32 v237, v190, v191\n"
  "v_cvt_pk_bf16_f32 v238, v192, v193\n"
  "v_cvt_pk_bf16_f32 v239, v194, v195\n"
  "v_pk_add_f32 v[52:53], v[188:189], v[192:193]\n"
  "s_waitcnt lgkmcnt(0)\n"
  "v_pk_add_f32 v[54:55], v[190:191], v[194:195]\n"
  "v_mfma_f32_16x16x32_bf16 v[92:95], v[116:119], v[236:239], v[92:95]\n"
  "v_pk_add_f32 v[220:221], v[220:221], v[222:223]\n"
  "v_pk_add_f32 v[224:225], v[224:225], v[226:227]\n"
  "v_mfma_f32_16x16x32_bf16 v[96:99], v[120:123], v[236:239], v[96:99]\n"
  "v_pk_add_f32 v[220:221], v[220:221], v[224:225]\n"
  "v_add_f32 v220, v220, v221\n"
  "v_mfma_f32_16x16x32_bf16 v[100:103], v[124:127], v[236:239], v[100:103]\n"
  "v_add_f32 v35, v35, v220\n"
  "v_mfma_f32_16x16x32_bf16 v[104:107], v[128:131], v[236:239], v[104:107]\n"
  "v_mfma_f32_16x16x32_bf16 v[108:111], v[132:135], v[236:239], v[108:111]\n"
  "v_mfma_f32_16x16x32_bf16 v[112:115], v[136:139], v[236:239], v[112:115]\n"
  ".Ldino_w_19:\n"
  "ds_read_b32 v8, v33 offset:152\n"
  "s_waitcnt lgkmcnt(0)\n"
  "v_cmp_ne_u32 vcc, 1, v8\n"
  "s_cbranch_vccnz .Ldino_w_19\n"
  "ds_read_b128 v[116:119], v27 offset:9216\n"
  "ds_read_b128 v[120:123], v28 offset:9216\n"
  "ds_read_b128 v[124:127], v27 offset:9344\n"
  "ds_read_b128 v[128:131], v28 offset:9344\n"
  "ds_read_b128 v[132:135], v27 offset:9472\n"
  "ds_read_b128 v[136:139], v28 offset:9472\n"
  "v_mov_b32 v184, v40\n"
  "v_mov_b32 v185, v40\n"
  "s_waitcnt lgkmcnt(5)\n"
  "v_mfma_f32_16x16x32_bf16 v[176:179], v[36:39], v[116:119], v[44:47]\n"
  "s_waitcnt lgkmcnt(4)\n"
  "v_mfma_f32_16x16x32_bf16 v[176:179], v[36:39], v[120:123], v[176:179]\n"
  "s_waitcnt lgkmcnt(3)\n"
  "v_mfma_f32_16x16x32_bf16 v[176:179], v[36:39], v[124:127], v[176:179]\n"
  "s_waitcnt lgkmcnt(2)\n"
  "v_mfma_f32_16x16x32_bf16 v[180:183], v[36:39], v[128:131], v[44:47]\n"
  "s_waitcnt lgkmcnt(1)\n"
  "v_mfma_f32_16x16x32_bf16 v[180:183], v[36:39], v[132:135], v[180:183]\n"
  "s_waitcnt lgkmcnt(0)\n"
  "v_mfma_f32_16x16x32_bf16 v[180:183], v[36:39], v[136:139], v[180:183]\n"
  "v_dot2c_f32_bf16 v184, v116, v116\n"
  "v_dot2c_f32_bf16 v184, v116, v56\n"
  "v_dot2c_f32_bf16 v185, v128, v128\n"
  "v_dot2c_f32_bf16 v185, v128, v56\n"
  "v_dot2c_f32_bf16 v184, v117, v117\n"
  "v_dot2c_f32_bf16 v184, v117, v57\n"
  "v_dot2c_f32_bf16 v185, v129, v129\n"
  "v_dot2c_f32_bf16 v185, v129, v57\n"
  "v_dot2c_f32_bf16 v184, v118, v118\n"
  "v_dot2c_f32_bf16 v184, v118, v58\n"
  "v_dot2c_f32_bf16 v185, v130, v130\n"
  "v_dot2c_f32_bf16 v185, v130, v58\n"
  "v_dot2c_f32_bf16 v184, v119, v119\n"
  "v_dot2c_f32_bf16 v184, v119, v59\n"
  "v_dot2c_f32_bf16 v185, v131, v131\n"
  "v_dot2c_f32_bf16 v185, v131, v59\n"
  "v_dot2c_f32_bf16 v184, v120, v120\n"
  "v_dot2c_f32_bf16 v184, v120, v60\n"
  "v_dot2c_f32_bf16 v185, v132, v132\n"
  "v_dot2c_f32_bf16 v185, v132, v60\n"
  "v_dot2c_f32_bf16 v184, v121, v121\n"
  "v_dot2c_f32_bf16 v184, v121, v61\n"
  "v_dot2c_f32_bf16 v185, v133, v133\n"
  "v_dot2c_f32_bf16 v185, v133, v61\n"
  "v_dot2c_f32_bf16 v184, v122, v122\n"
  "v_dot2c_f32_bf16 v184, v122, v62\n"
  "v_dot2c_f32_bf16 v185, v134, v134\n"
  "v_dot2c_f32_bf16 v185, v134, v62\n"
  "v_dot2c_f32_bf16 v184, v123, v123\n"
  "v_dot2c_f32_bf16 v184, v123, v63\n"
  "v_dot2c_f32_bf16 v185, v135, v135\n"
  "v_dot2c_f32_bf16 v185, v135, v63\n"
  "v_dot2c_f32_bf16 v184, v124, v124\n"
  "v_dot2c_f32_bf16 v184, v124, v64\n"
  "v_dot2c_f32_bf16 v185, v136, v136\n"
  "v_dot2c_f32_bf16 v185, v136, v64\n"
  "v_dot2c_f32_bf16 v184, v125, v125\n"
  "v_dot2c_f32_bf16 v184, v125, v65\n"
  "v_dot2c_f32_bf16 v185, v137, v137\n"
  "v_dot2c_f32_bf16 v185, v137, v65\n"
  "v_dot2c_f32_bf16 v184, v126, v126\n"
  "v_dot2c_f32_bf16 v184, v126, v66\n"
  "v_dot2c_f32_bf16 v185, v138, v138\n"
  "v_dot2c_f32_bf16 v185, v138, v66\n"
  "v_dot2c_f32_bf16 v184, v127, v127\n"
  "v_dot2c_f32_bf16 v184, v127, v67\n"
  "v_dot2c_f32_bf16 v185, v139, v139\n"
  "v_dot2c_f32_bf16 v185, v139, v67\n"
  "v_mfma_f32_16x16x32_bf16 v[188:191], v[116:119], v[80:83], v[48:51]\n"
  "v_cndmask_b32 v176, v176, v180, s[42:43]\n"
  "v_mfma_f32_16x16x32_bf16 v[188:191], v[120:123], v[84:87], v[188:191]\n"
  "v_permlane32_swap_b32 v184, v185\n"
  "v_add_f32 v184, v184, v185\n"
  "v_mfma_f32_16x16x32_bf16 v[188:191], v[124:127], v[88:91], v[188:191]\n"
  "v_mov_b32 v185, v184\n"
  "v_mfma_f32_16x16x32_bf16 v[192:195], v[128:131], v[80:83], v[48:51]\n"
  "s_nop 0\n"
  "v_permlane16_swap_b32 v184, v185\n"
  "v_add_f32 v184, v184, v185\n"
  "v_mfma_f32_16x16x32_bf16 v[192:195], v[132:135], v[84:87], v[192:195]\n"
  "v_fma_f32 v184, v184, s47, v41\n"
  "v_fma_f32 v184, -v176, v176, v184\n"
  "v_mfma_f32_16x16x32_bf16 v[192:195], v[136:139], v[88:91], v[192:195]\n"
  "v_max_f32 v184, v184, v41\n"
  "v_rsq_f32 v184, v184\n"
  "ds_read_b64_tr_b16 v[116:117], v29 offset:9216\n"
  "ds_read_b64_tr_b16 v[118:119], v31 offset:9344\n"
  "v_mfma_f32_16x16x4_f32 v[204:207], v184, v42, 0\n"
  "ds_read_b64_tr_b16 v[120:121], v30 offset:9216\n"
  "ds_read_b64_tr_b16 v[122:123], v32 offset:9344\n"
  "ds_read_b64_tr_b16 v[124:125], v31 offset:9216\n"
  "ds_read_b64_tr_b16 v[126:127], v29 offset:9472\n"
  "v_mfma_f32_16x16x4_f32 v[208:211], v184, v43, 0\n"
  "ds_read_b64_tr_b16 v[128:129], v32 offset:9216\n"
  "ds_read_b64_tr_b16 v[130:131], v30 offset:9472\n"
  "ds_read_b64_tr_b16 v[132:133], v29 offset:9344\n"
  "ds_read_b64_tr_b16 v[134:135], v31 offset:9472\n"
  "ds_read_b64_tr_b16 v[136:137], v30 offset:9344\n"
  "ds_read_b64_tr_b16 v[138:139], v32 offset:9472\n"
  "v_pk_fma_f32 v[188:189], v[188:189], v[204:205], v[34:35] op_sel_hi:[1,1,0] neg_lo:[0,0,1] neg_hi:[0,0,1]\n"
  "v_pk_fma_f32 v[190:191], v[190:191], v[206:207], v[34:35] op_sel_hi:[1,1,0] neg_lo:[0,0,1] neg_hi:[0,0,1]\n"
  "s_nop 1\n"
  "v_pk_fma_f32 v[192:193], v[192:193], v[208:209], v[34:35] op_sel_hi:[1,1,0] neg_lo:[0,0,1] neg_hi:[0,0,1]\n"
  "v_pk_fma_f32 v[194:195], v[194:195], v[210:211], v[34:35] op_sel_hi:[1,1,0] neg_lo:[0,0,1] neg_hi:[0,0,1]\n"
  "v_max3_f32 v244, v188, v189, v190\n"
  "v_max3_f32 v245, v191, v192, v193\n"
  "v_max3_f32 v244, v244, v194, v195\n"
  "v_max_f32 v244, v244, v245\n"
  "v_cmp_lt_f32 vcc, s48, v244\n"
  ".Ldino_redo1:\n"
  "v_exp_f32 v220, v188\n"
  "v_exp_f32 v221, v189\n"
  "v_exp_f32 v222, v190\n"
  "v_exp_f32 v223, v191\n"
  "v_exp_f32 v224, v192\n"
  "v_exp_f32 v225, v193\n"
  "v_exp_f32 v226, v194\n"
  "v_exp_f32 v227, v195\n"
  "s_cbranch_vccnz .Ldino_rare1\n"
  "v_pk_mul_f32 v[188:189], v[220:221], v[204:205]\n"
  "v_pk_mul_f32 v[190:191], v[222:223], v[206:207]\n"
  "v_pk_mul_f32 v[192:193], v[224:225], v[208:209]\n"
  "v_pk_mul_f32 v[194:195], v[226:227], v[210:211]\n"
  "v_cvt_pk_bf16_f32 v236, v188, v189\n"
  "v_cvt_pk_bf16_f32 v237, v190, v191\n"
  "v_cvt_pk_bf16_f32 v238, v192, v193\n"
  "v_cvt_pk_bf16_f32 v239, v194, v195\n"
  "v_pk_add_f32 v[188:189], v[188:189], v[192:193]\n"
  "s_waitcnt lgkmcnt(0)\n"
  "v_pk_add_f32 v[190:191], v[190:191], v[194:195]\n"
  "v_mfma_f32_16x16x32_bf16 v[92:95], v[116:119], v[236:239], v[92:95]\n"
  "v_pk_add_f32 v[52:53], v[52:53], v[188:189]\n"
  "v_pk_add_f32 v[54:55], v[54:55], v[190:191]\n"
  "v_mfma_f32_16x16x32_bf16 v[96:99], v[120:123], v[236:239], v[96:99]\n"
  "v_cvt_pk_bf16_f32 v52, v52, v53\n"
  "v_cvt_pk_bf16_f32 v53, v54, v55\n"
  "v_mfma_f32_16x16x32_bf16 v[100:103], v[124:127], v[236:239], v[100:103]\n"
  "v_pk_add_f32 v[220:221], v[220:221], v[222:223]\n"
  "v_pk_add_f32 v[224:225], v[224:225], v[226:227]\n"
  "v_mfma_f32_16x16x32_bf16 v[104:107], v[128:131], v[236:239], v[104:107]\n"
  "v_pk_add_f32 v[220:221], v[220:221], v[224:225]\n"
  "v_add_f32 v220, v220, v221\n"
  "v_mfma_f32_16x16x32_bf16 v[108:111], v[132:135], v[236:239], v[108:111]\n"
  "v_add_f32 v35, v35, v220\n"
  "v_mfma_f32_16x16x32_bf16 v[112:115], v[136:139], v[236:239], v[112:115]\n"
  "s_cmp_eq_u32 s22, 4\n"
  "s_cbranch_scc1 .Ldino_next\n"
  "v_mfma_f32_16x16x16_bf16 v[92:95], v[68:69], v[52:53], v[92:95]\n"
  "v_mov_b32 v8, v35\n"
  "v_add_u32 v21, s23, v18\n"
  "v_mfma_f32_16x16x16_bf16 v[96:99], v[70:71], v[52:53], v[96:99]\n"
  "v_permlane16_swap_b32 v35, v8\n"
  "v_lshl_add_u32 v22, v7, 3, s23\n"
  "v_mfma_f32_16x16x16_bf16 v[100:103], v[72:73], v[52:53], v[100:103]\n"
  "v_add_f32 v35, v35, v8\n"
  "v_mfma_f32_16x16x16_bf16 v[104:107], v[74:75], v[52:53], v[104:107]\n"
  "v_mov_b32 v8, v35\n"
  "v_mfma_f32_16x16x16_bf16 v[108:111], v[76:77], v[52:53], v[108:111]\n"
  "v_cvt_pk_bf16_f32 v116, v92, v93\n"
  "v_cvt_pk_bf16_f32 v117, v94, v95\n"
  "v_mfma_f32_16x16x16_bf16 v[112:115], v[78:79], v[52:53], v[112:115]\n"
  "v_permlane32_swap_b32 v35, v8\n"
  "v_cvt_pk_bf16_f32 v118, v96, v97\n"
  "v_cvt_pk_bf16_f32 v119, v98, v99\n"
  "v_add_f32 v35, v35, v8\n"
  "ds_write_b128 v21, v[116:119]\n"
  "v_cvt_pk_bf16_f32 v120, v100, v101\n"
  "v_cvt_pk_bf16_f32 v121, v102, v103\n"
  "v_cvt_pk_bf16_f32 v122, v104, v105\n"
  "v_cvt_pk_bf16_f32 v123, v106, v107\n"
  "ds_write_b64 v22, v[34:35] offset:3072\n"
  "ds_write_b128 v21, v[120:123] offset:1024\n"
  "v_cvt_pk_bf16_f32 v124, v108, v109\n"
  "v_cvt_pk_bf16_f32 v125, v110, v111\n"
  "v_cvt_pk_bf16_f32 v126, v112, v113\n"
  "v_cvt_pk_bf16_f32 v127, v114, v115\n"
  "ds_write_b128 v21, v[124:127] offset:2048\n"
  "ds_write_b32 v33, v5 offset:40\n"
  ".Ldino_red:\n"
  "s_cmp_gt_u32 s2, 127\n"
  "s_cbranch_scc1 .Ldino_end\n"
  "s_setprio 0\n"
  "s_memrealtime s[44:45]\n"
  "s_waitcnt lgkmcnt(0)\n"
  "s_sub_u32 s44, s44, s34\n"
  "s_sub_u32 s44, 760, s44\n"
  "s_cbranch_scc1 .Ldino_go\n"
  ".Ldino_slp:\n"
  "s_sleep 2\n"
  "s_sub_u32 s44, s44, 6\n"
  "s_cbranch_scc0 .Ldino_slp\n"
  ".Ldino_go:\n"
  "s_cmp_lt_u32 s3, 3\n"
  "s_cbranch_scc1 .Ldino_go1\n"
  ".Ldino_w_21:\n"
  "ds_read_b32 v8, v4 offset:36\n"
  "s_sleep 1\n"
  "s_waitcnt lgkmcnt(0)\n"
  "v_cmp_ne_u32 vcc, 1, v8\n"
  "s_cbranch_vccnz .Ldino_w_21\n"
  "ds_read_b32 v250, v250 offset:448\n"
  ".Ldino_go1:\n"
  "s_mov_b32 s52, 0x10000\n"
  ".Ldino_poll:\n"
  "buffer_load_dwordx4 v[28:31], v248, s[24:27], 0 offen sc1\n"
  "buffer_load_dwordx4 v[32:35], v248, s[24:27], s54 offen sc1\n"
  "s_sub_u32 s52, s52, 1\n"
  "s_cbranch_scc1 .Ldino_polled\n"
  "s_waitcnt vmcnt(1)\n"
  "v_xor_b32 v28, s53, v28\n"
  "v_xor_b32 v29, s53, v29\n"
  "v_xor_b32 v30, s53, v30\n"
  "v_xor_b32 v31, s53, v31\n"
  "v_or3_b32 v8, v28, v29, v30\n"
  "s_waitcnt vmcnt(0)\n"
  "v_xor_b32 v32, s53, v32\n"
  "v_xor_b32 v33, s53, v33\n"
  "v_xor_b32 v34, s53, v34\n"
  "v_xor_b32 v35, s53, v35\n"
  "v_or3_b32 v8, v8, v31, v32\n"
  "v_or3_b32 v9, v33, v34, v35\n"
  "v_bitop3_b32 v8, v8, v9, s55 bitop3:0xa8\n"
  "v_cmp_ne_u32 vcc, 0, v8\n"
  "s_and_b64 exec, exec, vcc\n"
  "s_cbranch_execnz .Ldino_poll\n"
  ".Ldino_polled:\n"
  "s_mov_b64 exec, -1\n"
  "s_waitcnt vmcnt(0)\n"
  "v_perm_b32 v8, v29, v30, s56\n"
  "v_perm_b32 v9, v33, v34, s56\n"
  ".Ldino_redo2:\n"
  "s_waitcnt lgkmcnt(0)\n"
  "v_sub_f32 v10, v8, v250\n"
  "v_sub_f32 v11, v9, v250\n"
  "v_max_f32 v12, v10, v11\n"
  "v_cmp_lt_f32 vcc, s57, v12\n"
  "v_exp_f32 v10, v10\n"
  "v_exp_f32 v11, v11\n"
  "s_and_b64 vcc, vcc, s[58:59]\n"
  "s_cbranch_scc1 .Ldino_rare2\n"
  "v_mul_f32_dpp v28, v10, v28 quad_perm:[3,3,3,3] row_mask:0xf bank_mask:0xf bound_ctrl:1\n"
  "v_mul_f32_dpp v29, v10, v29 quad_perm:[3,3,3,3] row_mask:0xf bank_mask:0xf bound_ctrl:1\n"
  "v_mul_f32_dpp v30, v10, v30 quad_perm:[3,3,3,3] row_mask:0xf bank_mask:0xf bound_ctrl:1\n"
  "v_mul_f32_dpp v31, v10, v31 quad_perm:[3,3,3,3] row_mask:0xf bank_mask:0xf bound_ctrl:1\n"
  "v_fmac_f32_dpp v28, v11, v32 quad_perm:[3,3,3,3] row_mask:0xf bank_mask:0xf bound_ctrl:1\n"
  "v_fmac_f32_dpp v29, v11, v33 quad_perm:[3,3,3,3] row_mask:0xf bank_mask:0xf bound_ctrl:1\n"
  "v_fmac_f32_dpp v30, v11, v34 quad_perm:[3,3,3,3] row_mask:0xf bank_mask:0xf bound_ctrl:1\n"
  "v_fmac_f32_dpp v31, v11, v35 quad_perm:[3,3,3,3] row_mask:0xf bank_mask:0xf bound_ctrl:1\n"
  "v_permlane32_swap_b32 v28, v29\n"
  "ds_write_b32 v253, v250 offset:1024\n"
  "v_permlane32_swap_b32 v30, v31\n"
  "v_add_f32 v28, v28, v29\n"
  "v_add_f32 v30, v30, v31\n"
  "s_nop 1\n"
  "v_permlane16_swap_b32 v28, v30\n"
  "v_add_f32 v28, v28, v30\n"
  "s_nop 1\n"
  "v_add_f32_dpp v28, v28, v28 row_ror:4 row_mask:0xf bank_mask:0xf bound_ctrl:1\n"
  "s_cmp_lg_u32 s3, 3\n"
  "s_nop 0\n"
  "v_add_f32_dpp v28, v28, v28 row_ror:8 row_mask:0xf bank_mask:0xf bound_ctrl:1\n"
  "ds_write_b32 v249, v28 offset:512\n"
  "s_waitcnt lgkmcnt(0)\n"
  "s_barrier\n"
  "s_cbranch_scc1 .Ldino_end\n"
  "s_mov_b64 exec, 0xffff\n"
  "ds_read_b32 v8, v4 offset:188\n"
  "ds_read_b128 v[28:31], v254 offset:512\n"
  "ds_read_b128 v[32:35], v254 offset:528\n"
  "s_waitcnt lgkmcnt(2)\n"
  "v_cmp_ne_u32 vcc, 0, v8\n"
  "s_cbranch_vccnz .Ldino_rare3\n"
  ".Ldino_fin:\n"
  "s_waitcnt lgkmcnt(1)\n"
  "v_pk_add_f32 v[28:29], v[28:29], v[30:31]\n"
  "s_waitcnt lgkmcnt(0)\n"
  "v_pk_add_f32 v[32:33], v[32:33], v[34:35]\n"
  "v_pk_add_f32 v[28:29], v[28:29], v[32:33]\n"
  "v_add_f32 v28, v28, v29\n"
  "s_nop 1\n"
  "v_rcp_f32_dpp v29, v28 row_newbcast:3 row_mask:0xf bank_mask:0xf bound_ctrl:1\n"
  "s_mov_b64 exec, 0x7777\n"
  "v_fmac_f32 v251, v28, v29\n"
  "v_cvt_pk_bf16_f32 v251, v251, v251\n"
  "global_store_short v252, v251, s[18:19]\n"
  "s_cmp_lg_u32 s2, 0\n"
  "s_cbranch_scc1 .Ldino_end\n"
  "s_mov_b64 exec, 1\n"
  "buffer_store_dword v15, off, s[24:27], 0\n"
  ".Ldino_end:\n"
  "s_endpgm\n"
  ".Ldino_ldone:\n"
  "s_mov_b64 exec, 0x7777\n"
  "global_load_ushort v251, v252, s[4:5]\n"
  "s_mov_b64 exec, -1\n"
  "ds_read_b64 v[14:15], v4 offset:192\n"
  "v_lshlrev_b32 v254, 5, v0\n"
  "s_waitcnt lgkmcnt(0)\n"
  "v_readfirstlane_b32 s53, v14\n"
  "s_waitcnt vmcnt(0)\n"
  "v_lshlrev_b32 v251, 16, v251\n"
  "s_branch .Ldino_red\n"
  ".Ldino_rare0:\n"
  "s_mov_b32 s35, 0\n"
  "s_branch .Ldino_rare\n"
  ".Ldino_rare1:\n"
  "s_mov_b32 s35, 1\n"
  ".Ldino_rare:\n"
  "v_mov_b32 v245, v244\n"
  "s_nop 1\n"
  "v_permlane16_swap_b32 v244, v245\n"
  "v_max_f32 v244, v244, v245\n"
  "v_mov_b32 v245, v244\n"
  "s_nop 1\n"
  "v_permlane32_swap_b32 v244, v245\n"
  "v_max_f32 v244, v244, v245\n"
  "v_max_f32 v244, 0, v244\n"
  "v_exp_f32 v246, -v244\n"
  "v_add_f32 v34, v34, v244\n"
  "v_sub_f32 v188, v188, v244\n"
  "v_sub_f32 v189, v189, v244\n"
  "v_sub_f32 v190, v190, v244\n"
  "v_sub_f32 v191, v191, v244\n"
  "v_sub_f32 v192, v192, v244\n"
  "v_sub_f32 v193, v193, v244\n"
  "v_sub_f32 v194, v194, v244\n"
  "v_sub_f32 v195, v195, v244\n"
  "v_mul_f32 v35, v35, v246\n"
  "v_mul_f32 v52, v52, v246\n"
  "v_mul_f32 v53, v53, v246\n"
  "v_mul_f32 v54, v54, v246\n"
  "v_mul_f32 v55, v55, v246\n"
  "v_mul_f32 v92, v92, v246\n"
  "v_mul_f32 v93, v93, v246\n"
  "v_mul_f32 v94, v94, v246\n"
  "v_mul_f32 v95, v95, v246\n"
  "v_mul_f32 v96, v96, v246\n"
  "v_mul_f32 v97, v97, v246\n"
  "v_mul_f32 v98, v98, v246\n"
  "v_mul_f32 v99, v99, v246\n"
  "v_mul_f32 v100, v100, v246\n"
  "v_mul_f32 v101, v101, v246\n"
  "v_mul_f32 v102, v102, v246\n"
  "v_mul_f32 v103, v103, v246\n"
  "v_mul_f32 v104, v104, v246\n"
  "v_mul_f32 v105, v105, v246\n"
  "v_mul_f32 v106, v106, v246\n"
  "v_mul_f32 v107, v107, v246\n"
  "v_mul_f32 v108, v108, v246\n"
  "v_mul_f32 v109, v109, v246\n"
  "v_mul_f32 v110, v110, v246\n"
  "v_mul_f32 v111, v111, v246\n"
  "v_mul_f32 v112, v112, v246\n"
  "v_mul_f32 v113, v113, v246\n"
  "v_mul_f32 v114, v114, v246\n"
  "v_mul_f32 v115, v115, v246\n"
  "s_mov_b64 vcc, 0\n"
  "s_cmp_eq_u32 s35, 0\n"
  "s_cbranch_scc1 .Ldino_redo0\n"
  "s_branch .Ldino_redo1\n"
  ".Ldino_rare2:\n"
  "v_max_f32 v250, v8, v9\n"
  "s_nop 1\n"
  "v_mov_b32_dpp v250, v250 quad_perm:[3,3,3,3] row_mask:0xf bank_mask:0xf bound_ctrl:1\n"
  "s_nop 1\n"
  "v_max_f32_dpp v250, v250, v250 row_ror:4 row_mask:0xf bank_mask:0xf bound_ctrl:1\n"
  "s_nop 1\n"
  "v_max_f32_dpp v250, v250, v250 row_ror:8 row_mask:0xf bank_mask:0xf bound_ctrl:1\n"
  "v_mov_b32 v12, v250\n"
  "s_nop 1\n"
  "v_permlane16_swap_b32 v250, v12\n"
  "v_max_f32 v250, v250, v12\n"
  "v_mov_b32 v12, v250\n"
  "s_nop 1\n"
  "v_permlane32_swap_b32 v250, v12\n"
  "v_max_f32 v250, v250, v12\n"
  "ds_write_b32 v4, v5 offset:188\n"
  "s_branch .Ldino_redo2\n"
  ".Ldino_rare3:\n"
  "ds_read_b128 v[40:43], v4 offset:1024\n"
  "ds_read_b128 v[44:47], v4 offset:1040\n"
  "s_waitcnt lgkmcnt(1)\n"
  "v_max3_f32 v48, v40, v41, v42\n"
  "s_waitcnt lgkmcnt(0)\n"
  "v_max3_f32 v48, v48, v43, v44\n"
  "v_max3_f32 v48, v48, v45, v46\n"
  "v_max_f32 v48, v48, v47\n"
  "v_sub_f32 v40, v40, v48\n"
  "v_exp_f32 v40, v40\n"
  "v_sub_f32 v41, v41, v48\n"
  "v_exp_f32 v41, v41\n"
  "v_sub_f32 v42, v42, v48\n"
  "v_exp_f32 v42, v42\n"
  "v_sub_f32 v43, v43, v48\n"
  "v_exp_f32 v43, v43\n"
  "v_sub_f32 v44, v44, v48\n"
  "v_exp_f32 v44, v44\n"
  "v_sub_f32 v45, v45, v48\n"
  "v_exp_f32 v45, v45\n"
  "v_sub_f32 v46, v46, v48\n"
  "v_exp_f32 v46, v46\n"
  "v_sub_f32 v47, v47, v48\n"
  "v_exp_f32 v47, v47\n"
  "v_mul_f32 v28, v28, v40\n"
  "v_mul_f32 v29, v29, v41\n"
  "v_mul_f32 v30, v30, v42\n"
  "v_mul_f32 v31, v31, v43\n"
  "v_mul_f32 v32, v32, v44\n"
  "v_mul_f32 v33, v33, v45\n"
  "v_mul_f32 v34, v34, v46\n"
  "v_mul_f32 v35, v35, v47\n"
  "s_branch .Ldino_fin\n"
  ".Ldino_helper:\n"
  "v_mov_b32 v20, s3\n"
  "v_lshl_add_u32 v30, v6, 3, v8\n"
  "v_mul_u32_u24 v30, 12, v30\n"
  "v_and_b32 v31, 1, v6\n"
  "v_lshlrev_b32 v31, 2, v31\n"
  "v_or_b32 v32, 2, v31\n"
  "v_and_b32 v9, 3, v7\n"
  "s_lshl_b32 s35, s3, 2\n"
  "v_add3_u32 v10, v30, s35, v9\n"
  "v_add_u32 v11, 48, v10\n"
  "v_xor_b32 v10, v10, v31\n"
  "v_xor_b32 v11, v11, v32\n"
  "s_mov_b32 s36, 101888\n"
  "v_lshl_add_u32 v21, v10, 4, s36\n"
  "v_lshl_add_u32 v22, v11, 4, s36\n"
  "s_lshl_b32 s35, s3, 10\n"
  "v_add_u32 v24, s35, v18\n"
  "s_lshl_b32 s35, s3, 6\n"
  "v_lshlrev_b32 v25, 2, v7\n"
  "v_add_u32 v23, s35, v25\n"
  "s_lshl_b32 s35, s3, 5\n"
  "v_lshl_add_u32 v10, v8, 3, v9\n"
  "v_add_u32 v10, s35, v10\n"
  "v_mad_u32_u24 v10, v10, 12, v6\n"
  "v_and_b32 v11, 1, v8\n"
  "v_lshlrev_b32 v11, 2, v11\n"
  "s_mov_b32 s36, 120320\n"
  "v_add_u32 v12, 0, v10\n"
  "v_xor_b32 v12, v12, v11\n"
  "v_lshl_add_u32 v26, v12, 4, s36\n"
  "v_add_u32 v12, 4, v10\n"
  "v_xor_b32 v12, v12, v11\n"
  "v_lshl_add_u32 v27, v12, 4, s36\n"
  "v_add_u32 v12, 48, v10\n"
  "v_xor_b32 v12, v12, v11\n"
  "v_xor_b32 v12, 2, v12\n"
  "v_lshl_add_u32 v28, v12, 4, s36\n"
  "v_add_u32 v12, 52, v10\n"
  "v_xor_b32 v12, v12, v11\n"
  "v_xor_b32 v12, 2, v12\n"
  "v_lshl_add_u32 v29, v12, 4, s36\n"
  "v_add_u32 v12, 157184, v16\n"
  "v_add_u32 v13, 157184, v17\n"
  "s_barrier\n"
  ".Ldino_w_23:\n"
  "ds_read_b32 v14, v4 offset:16\n"
  "s_sleep 1\n"
  "s_waitcnt lgkmcnt(0)\n"
  "v_cmp_ne_u32 vcc, 1, v14\n"
  "s_cbranch_vccnz .Ldino_w_23\n"
  "ds_read_b128 v[72:75], v12 offset:0\n"
  "ds_read_b128 v[76:79], v13 offset:0\n"
  "ds_read_b128 v[80:83], v12 offset:128\n"
  "s_waitcnt lgkmcnt(2)\n"
  "v_lshlrev_b32 v48, 16, v72\n"
  "v_and_b32 v49, 0xffff0000, v72\n"
  "v_lshlrev_b32 v50, 16, v73\n"
  "v_and_b32 v51, 0xffff0000, v73\n"
  "v_lshlrev_b32 v52, 16, v74\n"
  "v_and_b32 v53, 0xffff0000, v74\n"
  "v_lshlrev_b32 v54, 16, v75\n"
  "v_and_b32 v55, 0xffff0000, v75\n"
  "s_waitcnt lgkmcnt(1)\n"
  "v_lshlrev_b32 v56, 16, v76\n"
  "v_and_b32 v57, 0xffff0000, v76\n"
  "v_lshlrev_b32 v58, 16, v77\n"
  "v_and_b32 v59, 0xffff0000, v77\n"
  "v_lshlrev_b32 v60, 16, v78\n"
  "v_and_b32 v61, 0xffff0000, v78\n"
  "v_lshlrev_b32 v62, 16, v79\n"
  "v_and_b32 v63, 0xffff0000, v79\n"
  "s_waitcnt lgkmcnt(0)\n"
  "v_lshlrev_b32 v64, 16, v80\n"
  "v_and_b32 v65, 0xffff0000, v80\n"
  "v_lshlrev_b32 v66, 16, v81\n"
  "v_and_b32 v67, 0xffff0000, v81\n"
  "v_lshlrev_b32 v68, 16, v82\n"
  "v_and_b32 v69, 0xffff0000, v82\n"
  "v_lshlrev_b32 v70, 16, v83\n"
  "v_and_b32 v71, 0xffff0000, v83\n"
  "v_pk_add_f32 v[12:13], v[48:49], v[50:51]\n"
  "v_pk_add_f32 v[12:13], v[12:13], v[52:53]\n"
  "v_pk_add_f32 v[12:13], v[12:13], v[54:55]\n"
  "v_pk_add_f32 v[12:13], v[12:13], v[56:57]\n"
  "v_pk_add_f32 v[12:13], v[12:13], v[58:59]\n"
  "v_pk_add_f32 v[12:13], v[12:13], v[60:61]\n"
  "v_pk_add_f32 v[12:13], v[12:13], v[62:63]\n"
  "v_pk_add_f32 v[12:13], v[12:13], v[64:65]\n"
  "v_pk_add_f32 v[12:13], v[12:13], v[66:67]\n"
  "v_pk_add_f32 v[12:13], v[12:13], v[68:69]\n"
  "v_pk_add_f32 v[12:13], v[12:13], v[70:71]\n"
  "v_add_f32 v12, v12, v13\n"
  "v_mov_b32 v13, v12\n"
  "s_nop 1\n"
  "v_permlane16_swap_b32 v12, v13\n"
  "v_add_f32 v12, v12, v13\n"
  "v_mov_b32 v13, v12\n"
  "s_nop 1\n"
  "v_permlane32_swap_b32 v12, v13\n"
  "v_add_f32 v12, v12, v13\n"
  "v_mul_f32 v12, 0xbc2aaaab, v12\n"
  "v_pk_add_f32 v[48:49], v[48:49], v[12:13] op_sel_hi:[1,0]\n"
  "v_pk_add_f32 v[50:51], v[50:51], v[12:13] op_sel_hi:[1,0]\n"
  "v_pk_add_f32 v[52:53], v[52:53], v[12:13] op_sel_hi:[1,0]\n"
  "v_pk_add_f32 v[54:55], v[54:55], v[12:13] op_sel_hi:[1,0]\n"
  "v_pk_add_f32 v[56:57], v[56:57], v[12:13] op_sel_hi:[1,0]\n"
  "v_pk_add_f32 v[58:59], v[58:59], v[12:13] op_sel_hi:[1,0]\n"
  "v_pk_add_f32 v[60:61], v[60:61], v[12:13] op_sel_hi:[1,0]\n"
  "v_pk_add_f32 v[62:63], v[62:63], v[12:13] op_sel_hi:[1,0]\n"
  "v_pk_add_f32 v[64:65], v[64:65], v[12:13] op_sel_hi:[1,0]\n"
  "v_pk_add_f32 v[66:67], v[66:67], v[12:13] op_sel_hi:[1,0]\n"
  "v_pk_add_f32 v[68:69], v[68:69], v[12:13] op_sel_hi:[1,0]\n"
  "v_pk_add_f32 v[70:71], v[70:71], v[12:13] op_sel_hi:[1,0]\n"
  "v_pk_mul_f32 v[12:13], v[48:49], v[48:49]\n"
  "v_pk_fma_f32 v[12:13], v[50:51], v[50:51], v[12:13]\n"
  "v_pk_fma_f32 v[12:13], v[52:53], v[52:53], v[12:13]\n"
  "v_pk_fma_f32 v[12:13], v[54:55], v[54:55], v[12:13]\n"
  "v_pk_fma_f32 v[12:13], v[56:57], v[56:57], v[12:13]\n"
  "v_pk_fma_f32 v[12:13], v[58:59], v[58:59], v[12:13]\n"
  "v_pk_fma_f32 v[12:13], v[60:61], v[60:61], v[12:13]\n"
  "v_pk_fma_f32 v[12:13], v[62:63], v[62:63], v[12:13]\n"
  "v_pk_fma_f32 v[12:13], v[64:65], v[64:65], v[12:13]\n"
  "v_pk_fma_f32 v[12:13], v[66:67], v[66:67], v[12:13]\n"
  "v_pk_fma_f32 v[12:13], v[68:69], v[68:69], v[12:13]\n"
  "v_pk_fma_f32 v[12:13], v[70:71], v[70:71], v[12:13]\n"
  "v_add_f32 v12, v12, v13\n"
  "v_mov_b32 v13, v12\n"
  "s_nop 1\n"
  "v_permlane16_swap_b32 v12, v13\n"
  "v_add_f32 v12, v12, v13\n"
  "v_mov_b32 v13, v12\n"
  "s_nop 1\n"
  "v_permlane32_swap_b32 v12, v13\n"
  "v_add_f32 v12, v12, v13\n"
  "v_mov_b32 v13, 0x3727c5ac\n"
  "v_fmac_f32 v13, 0x3c2aaaab, v12\n"
  "v_rsq_f32 v12, v13\n"
  "s_nop 0\n"
  "v_pk_mul_f32 v[48:49], v[48:49], v[12:13] op_sel_hi:[1,0]\n"
  "v_pk_mul_f32 v[50:51], v[50:51], v[12:13] op_sel_hi:[1,0]\n"
  "v_pk_mul_f32 v[52:53], v[52:53], v[12:13] op_sel_hi:[1,0]\n"
  "v_pk_mul_f32 v[54:55], v[54:55], v[12:13] op_sel_hi:[1,0]\n"
  "v_pk_mul_f32 v[56:57], v[56:57], v[12:13] op_sel_hi:[1,0]\n"
  "v_pk_mul_f32 v[58:59], v[58:59], v[12:13] op_sel_hi:[1,0]\n"
  "v_pk_mul_f32 v[60:61], v[60:61], v[12:13] op_sel_hi:[1,0]\n"
  "v_pk_mul_f32 v[62:63], v[62:63], v[12:13] op_sel_hi:[1,0]\n"
  "v_pk_mul_f32 v[64:65], v[64:65], v[12:13] op_sel_hi:[1,0]\n"
  "v_pk_mul_f32 v[66:67], v[66:67], v[12:13] op_sel_hi:[1,0]\n"
  "v_pk_mul_f32 v[68:69], v[68:69], v[12:13] op_sel_hi:[1,0]\n"
  "v_pk_mul_f32 v[70:71], v[70:71], v[12:13] op_sel_hi:[1,0]\n"
  "v_cvt_pk_bf16_f32 v36, v48, v49\n"
  "v_cvt_pk_bf16_f32 v37, v50, v51\n"
  "v_cvt_pk_bf16_f32 v38, v52, v53\n"
  "v_cvt_pk_bf16_f32 v39, v54, v55\n"
  "v_cvt_pk_bf16_f32 v40, v56, v57\n"
  "v_cvt_pk_bf16_f32 v41, v58, v59\n"
  "v_cvt_pk_bf16_f32 v42, v60, v61\n"
  "v_cvt_pk_bf16_f32 v43, v62, v63\n"
  "v_cvt_pk_bf16_f32 v44, v64, v65\n"
  "v_cvt_pk_bf16_f32 v45, v66, v67\n"
  "v_cvt_pk_bf16_f32 v46, v68, v69\n"
  "v_cvt_pk_bf16_f32 v47, v70, v71\n"
  ".Ldino_w_25:\n"
  "ds_read_b32 v14, v4 offset:20\n"
  "s_waitcnt lgkmcnt(0)\n"
  "v_cmp_ne_u32 vcc, 1, v14\n"
  "s_cbranch_vccnz .Ldino_w_25\n"
  "ds_read_b64_tr_b16 v[48:49], v21 offset:0\n"
  "ds_read_b64_tr_b16 v[50:51], v22 offset:0\n"
  "ds_read_b64_tr_b16 v[52:53], v21 offset:8\n"
  "ds_read_b64_tr_b16 v[54:55], v22 offset:8\n"
  "ds_read_b64_tr_b16 v[56:57], v21 offset:6144\n"
  "ds_read_b64_tr_b16 v[58:59], v22 offset:6144\n"
  "ds_read_b64_tr_b16 v[60:61], v21 offset:6152\n"
  "ds_read_b64_tr_b16 v[62:63], v22 offset:6152\n"
  "ds_read_b64_tr_b16 v[64:65], v21 offset:12288\n"
  "ds_read_b64_tr_b16 v[66:67], v22 offset:12288\n"
  "ds_read_b64_tr_b16 v[68:69], v21 offset:12296\n"
  "ds_read_b64_tr_b16 v[70:71], v22 offset:12296\n"
  "s_waitcnt lgkmcnt(10)\n"
  "v_mfma_f32_16x16x32_bf16 v[72:75], v[48:51], v[36:39], 0\n"
  "s_waitcnt lgkmcnt(8)\n"
  "v_mfma_f32_16x16x32_bf16 v[76:79], v[52:55], v[36:39], 0\n"
  "s_waitcnt lgkmcnt(6)\n"
  "v_mfma_f32_16x16x32_bf16 v[72:75], v[56:59], v[40:43], v[72:75]\n"
  "s_waitcnt lgkmcnt(4)\n"
  "v_mfma_f32_16x16x32_bf16 v[76:79], v[60:63], v[40:43], v[76:79]\n"
  "s_waitcnt lgkmcnt(2)\n"
  "v_mfma_f32_16x16x32_bf16 v[72:75], v[64:67], v[44:47], v[72:75]\n"
  "s_waitcnt lgkmcnt(0)\n"
  "v_mfma_f32_16x16x32_bf16 v[76:79], v[68:71], v[44:47], v[76:79]\n"
  "s_nop 5\n"
  "v_cvt_pk_bf16_f32 v72, v72, v73\n"
  "v_cvt_pk_bf16_f32 v73, v74, v75\n"
  "v_cvt_pk_bf16_f32 v74, v76, v77\n"
  "v_cvt_pk_bf16_f32 v75, v78, v79\n"
  "ds_write_b128 v24, v[72:75] offset:512\n"
  "ds_write_b8 v20, v5 offset:0\n"
  ".Ldino_w_27:\n"
  "ds_read_b32 v14, v4 offset:0\n"
  "s_waitcnt lgkmcnt(0)\n"
  "v_cmp_ne_u32 vcc, 0x10101, v14\n"
  "s_cbranch_vccnz .Ldino_w_27\n"
  "ds_read_b128 v[36:39], v18 offset:512\n"
  "ds_read_b128 v[40:43], v18 offset:1536\n"
  "ds_read_b128 v[44:47], v18 offset:2560\n"
  ".Ldino_w_29:\n"
  "ds_read_b32 v14, v4 offset:24\n"
  "s_waitcnt lgkmcnt(0)\n"
  "v_cmp_ne_u32 vcc, 1, v14\n"
  "s_cbranch_vccnz .Ldino_w_29\n"
  "ds_read_b128 v[48:51], v26 offset:0\n"
  "ds_read_b128 v[52:55], v28 offset:0\n"
  "ds_read_b128 v[56:59], v27 offset:0\n"
  "ds_read_b128 v[60:63], v29 offset:0\n"
  "ds_read_b128 v[64:67], v26 offset:128\n"
  "ds_read_b128 v[68:71], v28 offset:128\n"
  "s_waitcnt lgkmcnt(5)\n"
  "v_mfma_f32_16x16x32_bf16 v[72:75], v[48:51], v[36:39], 0\n"
  "s_waitcnt lgkmcnt(4)\n"
  "v_mfma_f32_16x16x32_bf16 v[76:79], v[52:55], v[36:39], 0\n"
  "s_waitcnt lgkmcnt(3)\n"
  "v_mfma_f32_16x16x32_bf16 v[72:75], v[56:59], v[40:43], v[72:75]\n"
  "s_waitcnt lgkmcnt(2)\n"
  "v_mfma_f32_16x16x32_bf16 v[76:79], v[60:63], v[40:43], v[76:79]\n"
  "s_waitcnt lgkmcnt(1)\n"
  "v_mfma_f32_16x16x32_bf16 v[72:75], v[64:67], v[44:47], v[72:75]\n"
  "s_waitcnt lgkmcnt(0)\n"
  "v_mfma_f32_16x16x32_bf16 v[76:79], v[68:71], v[44:47], v[76:79]\n"
  "s_waitcnt vmcnt(0)\n"
  "v_add_u32 v19, 1, v19\n"
  "v_and_b32 v18, 0xfff, v19\n"
  "ds_write_b64 v4, v[18:19] offset:192\n"
  "v_readfirstlane_b32 s53, v18\n"
  "v_lshlrev_b32 v18, 4, v0\n"
  "v_pk_add_f32 v[12:13], v[72:73], v[74:75]\n"
  "s_nop 0\n"
  "v_pk_add_f32 v[14:15], v[76:77], v[78:79]\n"
  "v_pk_add_f32 v[12:13], v[12:13], v[14:15]\n"
  "v_add_f32 v12, v12, v13\n"
  "v_mov_b32 v13, v12\n"
  "s_nop 1\n"
  "v_permlane16_swap_b32 v12, v13\n"
  "v_add_f32 v12, v12, v13\n"
  "v_mov_b32 v13, v12\n"
  "s_nop 1\n"
  "v_permlane32_swap_b32 v12, v13\n"
  "v_add_f32 v12, v12, v13\n"
  "ds_write_b32 v23, v12 offset:256\n"
  "ds_write_b8 v20, v5 offset:4\n"
  "v_mov_b32 v10, 0x41622adf\n"
  "v_pk_mul_f32 v[72:73], v[72:73], v[10:11] op_sel_hi:[1,0]\n"
  "v_pk_mul_f32 v[74:75], v[74:75], v[10:11] op_sel_hi:[1,0]\n"
  "v_pk_mul_f32 v[76:77], v[76:77], v[10:11] op_sel_hi:[1,0]\n"
  "v_pk_mul_f32 v[78:79], v[78:79], v[10:11] op_sel_hi:[1,0]\n"
  ".Ldino_w_31:\n"
  "ds_read_b32 v14, v4 offset:4\n"
  "s_waitcnt lgkmcnt(0)\n"
  "v_cmp_ne_u32 vcc, 0x10101, v14\n"
  "s_cbranch_vccnz .Ldino_w_31\n"
  "ds_read_b32 v12, v25 offset:256\n"
  "ds_read_b32 v13, v25 offset:320\n"
  "ds_read_b32 v14, v25 offset:384\n"
  "s_waitcnt lgkmcnt(1)\n"
  "v_add_f32 v12, v12, v13\n"
  "s_waitcnt lgkmcnt(0)\n"
  "v_add_f32 v12, v12, v14\n"
  "v_mul_f32 v12, 0xbe16c740, v12\n"
  "v_pk_add_f32 v[72:73], v[72:73], v[12:13] op_sel_hi:[1,0]\n"
  "v_pk_add_f32 v[74:75], v[74:75], v[12:13] op_sel_hi:[1,0]\n"
  "v_pk_add_f32 v[76:77], v[76:77], v[12:13] op_sel_hi:[1,0]\n"
  "v_pk_add_f32 v[78:79], v[78:79], v[12:13] op_sel_hi:[1,0]\n"
  "v_cvt_pk_bf16_f32 v72, v72, v73\n"
  "v_cvt_pk_bf16_f32 v73, v74, v75\n"
  "v_cvt_pk_bf16_f32 v74, v76, v77\n"
  "v_cvt_pk_bf16_f32 v75, v78, v79\n"
  "ds_write_b128 v24, v[72:75] offset:3584\n"
  "ds_write_b8 v20, v5 offset:8\n"
  "s_setprio 0\n"
  "s_lshl_b32 s35, s2, 13\n"
  "s_mul_i32 s36, s3, 384\n"
  "s_add_u32 s35, s35, s36\n"
  "s_add_u32 s35, s35, 4096\n"
  "v_lshrrev_b32 v10, 1, v7\n"
  "v_and_b32 v11, 1, v7\n"
  "v_lshlrev_b32 v10, 10, v10\n"
  "v_lshl_add_u32 v10, v11, 6, v10\n"
  "v_lshl_add_u32 v10, v6, 4, v10\n"
  "v_add_u32 v33, s35, v10\n"
  "s_cmp_eq_u32 s3, 2\n"
  "s_cselect_b32 s35, 0x40000000, 256\n"
  "v_add_u32 v34, s35, v33\n"
  "s_mul_i32 s35, s3, 36\n"
  "s_mov_b32 s36, 138752\n"
  "v_lshl_add_u32 v10, v9, 2, s35\n"
  "v_add_u32 v10, 0, v10\n"
  "v_min_u32 v10, 92, v10\n"
  "v_lshrrev_b32 v11, 3, v10\n"
  "v_and_b32 v10, 4, v10\n"
  "v_lshl_add_u32 v10, v10, 1, s36\n"
  "v_add_u32 v11, v11, v30\n"
  "v_add_u32 v12, 48, v11\n"
  "v_xor_b32 v11, v11, v31\n"
  "v_xor_b32 v12, v12, v32\n"
  "v_lshl_add_u32 v144, v11, 4, v10\n"
  "v_lshl_add_u32 v145, v12, 4, v10\n"
  "v_lshl_add_u32 v10, v9, 2, s35\n"
  "v_add_u32 v10, 12, v10\n"
  "v_min_u32 v10, 92, v10\n"
  "v_lshrrev_b32 v11, 3, v10\n"
  "v_and_b32 v10, 4, v10\n"
  "v_lshl_add_u32 v10, v10, 1, s36\n"
  "v_add_u32 v11, v11, v30\n"
  "v_add_u32 v12, 48, v11\n"
  "v_xor_b32 v11, v11, v31\n"
  "v_xor_b32 v12, v12, v32\n"
  "v_lshl_add_u32 v146, v11, 4, v10\n"
  "v_lshl_add_u32 v147, v12, 4, v10\n"
  "v_lshl_add_u32 v10, v9, 2, s35\n"
  "v_add_u32 v10, 24, v10\n"
  "v_min_u32 v10, 92, v10\n"
  "v_lshrrev_b32 v11, 3, v10\n"
  "v_and_b32 v10, 4, v10\n"
  "v_lshl_add_u32 v10, v10, 1, s36\n"
  "v_add_u32 v11, v11, v30\n"
  "v_add_u32 v12, 48, v11\n"
  "v_xor_b32 v11, v11, v31\n"
  "v_xor_b32 v12, v12, v32\n"
  "v_lshl_add_u32 v148, v11, 4, v10\n"
  "v_lshl_add_u32 v149, v12, 4, v10\n"
  ".Ldino_w_33:\n"
  "ds_read_b32 v14, v4 offset:28\n"
  "s_sleep 2\n"
  "s_waitcnt lgkmcnt(0)\n"
  "v_cmp_ne_u32 vcc, 1, v14\n"
  "s_cbranch_vccnz .Ldino_w_33\n"
  "ds_read_b64_tr_b16 v[36:37], v144 offset:0\n"
  "ds_read_b64_tr_b16 v[38:39], v145 offset:0\n"
  "ds_read_b64_tr_b16 v[40:41], v144 offset:6144\n"
  "ds_read_b64_tr_b16 v[42:43], v145 offset:6144\n"
  "ds_read_b64_tr_b16 v[44:45], v144 offset:12288\n"
  "ds_read_b64_tr_b16 v[46:47], v145 offset:12288\n"
  "ds_read_b64_tr_b16 v[48:49], v146 offset:0\n"
  "ds_read_b64_tr_b16 v[50:51], v147 offset:0\n"
  "ds_read_b64_tr_b16 v[52:53], v146 offset:6144\n"
  "ds_read_b64_tr_b16 v[54:55], v147 offset:6144\n"
  "ds_read_b64_tr_b16 v[56:57], v146 offset:12288\n"
  "ds_read_b64_tr_b16 v[58:59], v147 offset:12288\n"
  "ds_read_b64_tr_b16 v[60:61], v148 offset:0\n"
  "ds_read_b64_tr_b16 v[62:63], v149 offset:0\n"
  "ds_read_b64_tr_b16 v[64:65], v148 offset:6144\n"
  "ds_read_b64_tr_b16 v[66:67], v149 offset:6144\n"
  "ds_read_b64_tr_b16 v[68:69], v148 offset:12288\n"
  "ds_read_b64_tr_b16 v[70:71], v149 offset:12288\n"
  "ds_write_b8 v20, v5 offset:12\n"
  ".Ldino_w_35:\n"
  "ds_read_b32 v14, v4 offset:32\n"
  "s_sleep 2\n"
  "s_waitcnt lgkmcnt(0)\n"
  "v_cmp_ne_u32 vcc, 1, v14\n"
  "s_cbranch_vccnz .Ldino_w_35\n"
  "ds_read_b128 v[144:147], v16 offset:6656\n"
  "ds_read_b128 v[148:151], v17 offset:6656\n"
  "ds_read_b128 v[152:155], v16 offset:6784\n"
  "ds_read_b128 v[156:159], v16 offset:9728\n"
  "ds_read_b128 v[160:163], v17 offset:9728\n"
  "ds_read_b128 v[164:167], v16 offset:9856\n"
  "s_waitcnt lgkmcnt(5)\n"
  "v_mfma_f32_16x16x32_bf16 v[72:75], v[144:147], v[36:39], 0\n"
  "v_mfma_f32_16x16x32_bf16 v[96:99], v[144:147], v[48:51], 0\n"
  "v_mfma_f32_16x16x32_bf16 v[120:123], v[144:147], v[60:63], 0\n"
  "s_waitcnt lgkmcnt(4)\n"
  "v_mfma_f32_16x16x32_bf16 v[72:75], v[148:151], v[40:43], v[72:75]\n"
  "v_mfma_f32_16x16x32_bf16 v[96:99], v[148:151], v[52:55], v[96:99]\n"
  "v_mfma_f32_16x16x32_bf16 v[120:123], v[148:151], v[64:67], v[120:123]\n"
  "s_waitcnt lgkmcnt(3)\n"
  "v_mfma_f32_16x16x32_bf16 v[72:75], v[152:155], v[44:47], v[72:75]\n"
  "v_mfma_f32_16x16x32_bf16 v[96:99], v[152:155], v[56:59], v[96:99]\n"
  "v_mfma_f32_16x16x32_bf16 v[120:123], v[152:155], v[68:71], v[120:123]\n"
  "ds_read_b128 v[144:147], v16 offset:12800\n"
  "ds_read_b128 v[148:151], v17 offset:12800\n"
  "ds_read_b128 v[152:155], v16 offset:12928\n"
  "s_waitcnt lgkmcnt(5)\n"
  "v_mfma_f32_16x16x32_bf16 v[76:79], v[156:159], v[36:39], 0\n"
  "v_mfma_f32_16x16x32_bf16 v[100:103], v[156:159], v[48:51], 0\n"
  "v_mfma_f32_16x16x32_bf16 v[124:127], v[156:159], v[60:63], 0\n"
  "s_waitcnt lgkmcnt(4)\n"
  "v_mfma_f32_16x16x32_bf16 v[76:79], v[160:163], v[40:43], v[76:79]\n"
  "v_mfma_f32_16x16x32_bf16 v[100:103], v[160:163], v[52:55], v[100:103]\n"
  "v_mfma_f32_16x16x32_bf16 v[124:127], v[160:163], v[64:67], v[124:127]\n"
  "s_waitcnt lgkmcnt(3)\n"
  "v_mfma_f32_16x16x32_bf16 v[76:79], v[164:167], v[44:47], v[76:79]\n"
  "v_mfma_f32_16x16x32_bf16 v[100:103], v[164:167], v[56:59], v[100:103]\n"
  "v_mfma_f32_16x16x32_bf16 v[124:127], v[164:167], v[68:71], v[124:127]\n"
  "ds_read_b128 v[156:159], v16 offset:15872\n"
  "ds_read_b128 v[160:163], v17 offset:15872\n"
  "ds_read_b128 v[164:167], v16 offset:16000\n"
  "s_waitcnt lgkmcnt(5)\n"
  "v_mfma_f32_16x16x32_bf16 v[80:83], v[144:147], v[36:39], 0\n"
  "v_mfma_f32_16x16x32_bf16 v[104:107], v[144:147], v[48:51], 0\n"
  "v_mfma_f32_16x16x32_bf16 v[128:131], v[144:147], v[60:63], 0\n"
  "s_waitcnt lgkmcnt(4)\n"
  "v_mfma_f32_16x16x32_bf16 v[80:83], v[148:151], v[40:43], v[80:83]\n"
  "v_mfma_f32_16x16x32_bf16 v[104:107], v[148:151], v[52:55], v[104:107]\n"
  "v_mfma_f32_16x16x32_bf16 v[128:131], v[148:151], v[64:67], v[128:131]\n"
  "s_waitcnt lgkmcnt(3)\n"
  "v_mfma_f32_16x16x32_bf16 v[80:83], v[152:155], v[44:47], v[80:83]\n"
  "v_mfma_f32_16x16x32_bf16 v[104:107], v[152:155], v[56:59], v[104:107]\n"
  "v_mfma_f32_16x16x32_bf16 v[128:131], v[152:155], v[68:71], v[128:131]\n"
  "ds_read_b128 v[144:147], v16 offset:18944\n"
  "ds_read_b128 v[148:151], v17 offset:18944\n"
  "ds_read_b128 v[152:155], v16 offset:19072\n"
  "s_waitcnt lgkmcnt(5)\n"
  "v_mfma_f32_16x16x32_bf16 v[84:87], v[156:159], v[36:39], 0\n"
  "v_mfma_f32_16x16x32_bf16 v[108:111], v[156:159], v[48:51], 0\n"
  "v_mfma_f32_16x16x32_bf16 v[132:135], v[156:159], v[60:63], 0\n"
  "s_waitcnt lgkmcnt(4)\n"
  "v_mfma_f32_16x16x32_bf16 v[84:87], v[160:163], v[40:43], v[84:87]\n"
  "v_mfma_f32_16x16x32_bf16 v[108:111], v[160:163], v[52:55], v[108:111]\n"
  "v_mfma_f32_16x16x32_bf16 v[132:135], v[160:163], v[64:67], v[132:135]\n"
  "s_waitcnt lgkmcnt(3)\n"
  "v_mfma_f32_16x16x32_bf16 v[84:87], v[164:167], v[44:47], v[84:87]\n"
  "v_mfma_f32_16x16x32_bf16 v[108:111], v[164:167], v[56:59], v[108:111]\n"
  "v_mfma_f32_16x16x32_bf16 v[132:135], v[164:167], v[68:71], v[132:135]\n"
  "ds_read_b128 v[156:159], v16 offset:22016\n"
  "ds_read_b128 v[160:163], v17 offset:22016\n"
  "ds_read_b128 v[164:167], v16 offset:22144\n"
  "s_waitcnt lgkmcnt(5)\n"
  "v_mfma_f32_16x16x32_bf16 v[88:91], v[144:147], v[36:39], 0\n"
  "v_mfma_f32_16x16x32_bf16 v[112:115], v[144:147], v[48:51], 0\n"
  "v_mfma_f32_16x16x32_bf16 v[136:139], v[144:147], v[60:63], 0\n"
  "s_waitcnt lgkmcnt(4)\n"
  "v_mfma_f32_16x16x32_bf16 v[88:91], v[148:151], v[40:43], v[88:91]\n"
  "v_mfma_f32_16x16x32_bf16 v[112:115], v[148:151], v[52:55], v[112:115]\n"
  "v_mfma_f32_16x16x32_bf16 v[136:139], v[148:151], v[64:67], v[136:139]\n"
  "s_waitcnt lgkmcnt(3)\n"
  "v_mfma_f32_16x16x32_bf16 v[88:91], v[152:155], v[44:47], v[88:91]\n"
  "v_mfma_f32_16x16x32_bf16 v[112:115], v[152:155], v[56:59], v[112:115]\n"
  "v_mfma_f32_16x16x32_bf16 v[136:139], v[152:155], v[68:71], v[136:139]\n"
  "s_waitcnt lgkmcnt(2)\n"
  "v_mfma_f32_16x16x32_bf16 v[92:95], v[156:159], v[36:39], 0\n"
  "v_mfma_f32_16x16x32_bf16 v[116:119], v[156:159], v[48:51], 0\n"
  "v_mfma_f32_16x16x32_bf16 v[140:143], v[156:159], v[60:63], 0\n"
  "s_waitcnt lgkmcnt(1)\n"
  "v_mfma_f32_16x16x32_bf16 v[92:95], v[160:163], v[40:43], v[92:95]\n"
  "v_mfma_f32_16x16x32_bf16 v[116:119], v[160:163], v[52:55], v[116:119]\n"
  "v_mfma_f32_16x16x32_bf16 v[140:143], v[160:163], v[64:67], v[140:143]\n"
  "s_waitcnt lgkmcnt(0)\n"
  "v_mfma_f32_16x16x32_bf16 v[92:95], v[164:167], v[44:47], v[92:95]\n"
  "v_mfma_f32_16x16x32_bf16 v[116:119], v[164:167], v[56:59], v[116:119]\n"
  "v_mfma_f32_16x16x32_bf16 v[140:143], v[164:167], v[68:71], v[140:143]\n"
  "v_pk_add_f32 v[10:11], v[72:73], v[74:75]\n"
  "v_pk_add_f32 v[10:11], v[10:11], v[76:77]\n"
  "v_pk_add_f32 v[10:11], v[10:11], v[78:79]\n"
  "v_pk_add_f32 v[10:11], v[10:11], v[80:81]\n"
  "v_pk_add_f32 v[10:11], v[10:11], v[82:83]\n"
  "v_pk_add_f32 v[10:11], v[10:11], v[84:85]\n"
  "v_pk_add_f32 v[10:11], v[10:11], v[86:87]\n"
  "v_pk_add_f32 v[10:11], v[10:11], v[88:89]\n"
  "v_pk_add_f32 v[10:11], v[10:11], v[90:91]\n"
  "v_pk_add_f32 v[10:11], v[10:11], v[92:93]\n"
  "v_pk_add_f32 v[10:11], v[10:11], v[94:95]\n"
  "v_add_f32 v10, v10, v11\n"
  "v_mov_b32 v11, v10\n"
  "s_nop 1\n"
  "v_permlane16_swap_b32 v10, v11\n"
  "v_add_f32 v10, v10, v11\n"
  "v_mov_b32 v11, v10\n"
  "s_nop 1\n"
  "v_permlane32_swap_b32 v10, v11\n"
  "v_add_f32 v10, v10, v11\n"
  "v_mul_f32 v10, 0xbc2aaaab, v10\n"
  "v_pk_add_f32 v[72:73], v[72:73], v[10:11] op_sel_hi:[1,0]\n"
  "v_pk_add_f32 v[74:75], v[74:75], v[10:11] op_sel_hi:[1,0]\n"
  "v_pk_add_f32 v[76:77], v[76:77], v[10:11] op_sel_hi:[1,0]\n"
  "v_pk_add_f32 v[78:79], v[78:79], v[10:11] op_sel_hi:[1,0]\n"
  "v_pk_add_f32 v[80:81], v[80:81], v[10:11] op_sel_hi:[1,0]\n"
  "v_pk_add_f32 v[82:83], v[82:83], v[10:11] op_sel_hi:[1,0]\n"
  "v_pk_add_f32 v[84:85], v[84:85], v[10:11] op_sel_hi:[1,0]\n"
  "v_pk_add_f32 v[86:87], v[86:87], v[10:11] op_sel_hi:[1,0]\n"
  "v_pk_add_f32 v[88:89], v[88:89], v[10:11] op_sel_hi:[1,0]\n"
  "v_pk_add_f32 v[90:91], v[90:91], v[10:11] op_sel_hi:[1,0]\n"
  "v_pk_add_f32 v[92:93], v[92:93], v[10:11] op_sel_hi:[1,0]\n"
  "v_pk_add_f32 v[94:95], v[94:95], v[10:11] op_sel_hi:[1,0]\n"
  "v_cvt_pk_bf16_f32 v36, v72, v73\n"
  "v_cvt_pk_bf16_f32 v37, v74, v75\n"
  "v_cvt_pk_bf16_f32 v38, v76, v77\n"
  "v_cvt_pk_bf16_f32 v39, v78, v79\n"
  "v_cvt_pk_bf16_f32 v40, v80, v81\n"
  "v_cvt_pk_bf16_f32 v41, v82, v83\n"
  "v_cvt_pk_bf16_f32 v42, v84, v85\n"
  "v_cvt_pk_bf16_f32 v43, v86, v87\n"
  "v_cvt_pk_bf16_f32 v44, v88, v89\n"
  "v_cvt_pk_bf16_f32 v45, v90, v91\n"
  "v_cvt_pk_bf16_f32 v46, v92, v93\n"
  "v_cvt_pk_bf16_f32 v47, v94, v95\n"
  "v_pk_add_f32 v[10:11], v[96:97], v[98:99]\n"
  "v_pk_add_f32 v[10:11], v[10:11], v[100:101]\n"
  "v_pk_add_f32 v[10:11], v[10:11], v[102:103]\n"
  "v_pk_add_f32 v[10:11], v[10:11], v[104:105]\n"
  "v_pk_add_f32 v[10:11], v[10:11], v[106:107]\n"
  "v_pk_add_f32 v[10:11], v[10:11], v[108:109]\n"
  "v_pk_add_f32 v[10:11], v[10:11], v[110:111]\n"
  "v_pk_add_f32 v[10:11], v[10:11], v[112:113]\n"
  "v_pk_add_f32 v[10:11], v[10:11], v[114:115]\n"
  "v_pk_add_f32 v[10:11], v[10:11], v[116:117]\n"
  "v_pk_add_f32 v[10:11], v[10:11], v[118:119]\n"
  "v_add_f32 v10, v10, v11\n"
  "v_mov_b32 v11, v10\n"
  "s_nop 1\n"
  "v_permlane16_swap_b32 v10, v11\n"
  "v_add_f32 v10, v10, v11\n"
  "v_mov_b32 v11, v10\n"
  "s_nop 1\n"
  "v_permlane32_swap_b32 v10, v11\n"
  "v_add_f32 v10, v10, v11\n"
  "v_mul_f32 v10, 0xbc2aaaab, v10\n"
  "v_pk_add_f32 v[96:97], v[96:97], v[10:11] op_sel_hi:[1,0]\n"
  "v_pk_add_f32 v[98:99], v[98:99], v[10:11] op_sel_hi:[1,0]\n"
  "v_pk_add_f32 v[100:101], v[100:101], v[10:11] op_sel_hi:[1,0]\n"
  "v_pk_add_f32 v[102:103], v[102:103], v[10:11] op_sel_hi:[1,0]\n"
  "v_pk_add_f32 v[104:105], v[104:105], v[10:11] op_sel_hi:[1,0]\n"
  "v_pk_add_f32 v[106:107], v[106:107], v[10:11] op_sel_hi:[1,0]\n"
  "v_pk_add_f32 v[108:109], v[108:109], v[10:11] op_sel_hi:[1,0]\n"
  "v_pk_add_f32 v[110:111], v[110:111], v[10:11] op_sel_hi:[1,0]\n"
  "v_pk_add_f32 v[112:113], v[112:113], v[10:11] op_sel_hi:[1,0]\n"
  "v_pk_add_f32 v[114:115], v[114:115], v[10:11] op_sel_hi:[1,0]\n"
  "v_pk_add_f32 v[116:117], v[116:117], v[10:11] op_sel_hi:[1,0]\n"
  "v_pk_add_f32 v[118:119], v[118:119], v[10:11] op_sel_hi:[1,0]\n"
  "v_cvt_pk_bf16_f32 v48, v96, v97\n"
  "v_cvt_pk_bf16_f32 v49, v98, v99\n"
  "v_cvt_pk_bf16_f32 v50, v100, v101\n"
  "v_cvt_pk_bf16_f32 v51, v102, v103\n"
  "v_cvt_pk_bf16_f32 v52, v104, v105\n"
  "v_cvt_pk_bf16_f32 v53, v106, v107\n"
  "v_cvt_pk_bf16_f32 v54, v108, v109\n"
  "v_cvt_pk_bf16_f32 v55, v110, v111\n"
  "v_cvt_pk_bf16_f32 v56, v112, v113\n"
  "v_cvt_pk_bf16_f32 v57, v114, v115\n"
  "v_cvt_pk_bf16_f32 v58, v116, v117\n"
  "v_cvt_pk_bf16_f32 v59, v118, v119\n"
  "v_pk_add_f32 v[10:11], v[120:121], v[122:123]\n"
  "v_pk_add_f32 v[10:11], v[10:11], v[124:125]\n"
  "v_pk_add_f32 v[10:11], v[10:11], v[126:127]\n"
  "v_pk_add_f32 v[10:11], v[10:11], v[128:129]\n"
  "v_pk_add_f32 v[10:11], v[10:11], v[130:131]\n"
  "v_pk_add_f32 v[10:11], v[10:11], v[132:133]\n"
  "v_pk_add_f32 v[10:11], v[10:11], v[134:135]\n"
  "v_pk_add_f32 v[10:11], v[10:11], v[136:137]\n"
  "v_pk_add_f32 v[10:11], v[10:11], v[138:139]\n"
  "v_pk_add_f32 v[10:11], v[10:11], v[140:141]\n"
  "v_pk_add_f32 v[10:11], v[10:11], v[142:143]\n"
  "v_add_f32 v10, v10, v11\n"
  "v_mov_b32 v11, v10\n"
  "s_nop 1\n"
  "v_permlane16_swap_b32 v10, v11\n"
  "v_add_f32 v10, v10, v11\n"
  "v_mov_b32 v11, v10\n"
  "s_nop 1\n"
  "v_permlane32_swap_b32 v10, v11\n"
  "v_add_f32 v10, v10, v11\n"
  "v_mul_f32 v10, 0xbc2aaaab, v10\n"
  "v_pk_add_f32 v[120:121], v[120:121], v[10:11] op_sel_hi:[1,0]\n"
  "v_pk_add_f32 v[122:123], v[122:123], v[10:11] op_sel_hi:[1,0]\n"
  "v_pk_add_f32 v[124:125], v[124:125], v[10:11] op_sel_hi:[1,0]\n"
  "v_pk_add_f32 v[126:127], v[126:127], v[10:11] op_sel_hi:[1,0]\n"
  "v_pk_add_f32 v[128:129], v[128:129], v[10:11] op_sel_hi:[1,0]\n"
  "v_pk_add_f32 v[130:131], v[130:131], v[10:11] op_sel_hi:[1,0]\n"
  "v_pk_add_f32 v[132:133], v[132:133], v[10:11] op_sel_hi:[1,0]\n"
  "v_pk_add_f32 v[134:135], v[134:135], v[10:11] op_sel_hi:[1,0]\n"
  "v_pk_add_f32 v[136:137], v[136:137], v[10:11] op_sel_hi:[1,0]\n"
  "v_pk_add_f32 v[138:139], v[138:139], v[10:11] op_sel_hi:[1,0]\n"
  "v_pk_add_f32 v[140:141], v[140:141], v[10:11] op_sel_hi:[1,0]\n"
  "v_pk_add_f32 v[142:143], v[142:143], v[10:11] op_sel_hi:[1,0]\n"
  "v_cvt_pk_bf16_f32 v60, v120, v121\n"
  "v_cvt_pk_bf16_f32 v61, v122, v123\n"
  "v_cvt_pk_bf16_f32 v62, v124, v125\n"
  "v_cvt_pk_bf16_f32 v63, v126, v127\n"
  "v_cvt_pk_bf16_f32 v64, v128, v129\n"
  "v_cvt_pk_bf16_f32 v65, v130, v131\n"
  "v_cvt_pk_bf16_f32 v66, v132, v133\n"
  "v_cvt_pk_bf16_f32 v67, v134, v135\n"
  "v_cvt_pk_bf16_f32 v68, v136, v137\n"
  "v_cvt_pk_bf16_f32 v69, v138, v139\n"
  "v_cvt_pk_bf16_f32 v70, v140, v141\n"
  "v_cvt_pk_bf16_f32 v71, v142, v143\n"
  "v_mov_b32 v125, 0\n"
  "v_add_u32 v21, 101888, v18\n"
  "v_lshlrev_b32 v22, 3, v7\n"
  "v_add_u32 v22, 104960, v22\n"
  "v_mov_b32 v23, 60\n"
  "s_mov_b32 s46, 0xfffff000\n"
  "s_mov_b32 s47, 0xffff0000\n"
  "s_mov_b32 s48, -1\n"
  "s_mov_b32 s49, 0xffff\n"
  "s_mov_b32 s50, 0x41a00000\n"
  "s_mov_b32 s35, 0\n"
  "v_mov_b32 v24, s53\n"
  "v_mov_b32 v115, s53\n"
  "v_mov_b32 v119, s53\n"
  "v_mov_b32 v123, s53\n"
  "v_mov_b64 v[100:101], 0\n"
  "v_mov_b64 v[102:103], 0\n"
  "v_mov_b64 v[104:105], 0\n"
  "v_mov_b64 v[106:107], 0\n"
  "v_mov_b64 v[108:109], 0\n"
  "v_mov_b64 v[110:111], 0\n"
  ".Ldino_w_37:\n"
  "ds_read_b32 v14, v23 offset:0\n"
  "s_sleep 2\n"
  "s_waitcnt lgkmcnt(0)\n"
  "v_cmp_ne_u32 vcc, 1, v14\n"
  "s_cbranch_vccnz .Ldino_w_37\n"
  "s_setprio 2\n"
  "ds_read_b32 v86, v23 offset:0\n"
  "ds_read_b64 v[84:85], v22 offset:0\n"
  "ds_read_b128 v[72:75], v21 offset:0\n"
  "ds_read_b128 v[76:79], v21 offset:1024\n"
  "ds_read_b128 v[80:83], v21 offset:2048\n"
  "s_waitcnt lgkmcnt(3)\n"
  "v_mov_b32 v124, v84\n"
  "ds_write_b32 v25, v124 offset:448\n"
  "ds_write_b32 v4, v5 offset:36\n"
  "ds_read_b32 v250, v250 offset:448\n"
  ".Ldino_hp:\n"
  "s_waitcnt lgkmcnt(3)\n"
  "v_sub_f32 v126, v84, v124\n"
  "v_cmp_lt_f32 vcc, s50, v126\n"
  "s_waitcnt lgkmcnt(2)\n"
  "v_mfma_f32_16x16x32_bf16 v[88:91], v[36:39], v[72:75], 0\n"
  "s_cbranch_vccnz .Ldino_hrare0\n"
  ".Ldino_hback0:\n"
  "v_exp_f32 v126, v126\n"
  "s_waitcnt lgkmcnt(1)\n"
  "v_mfma_f32_16x16x32_bf16 v[88:91], v[40:43], v[76:79], v[88:91]\n"
  "s_waitcnt lgkmcnt(0)\n"
  "v_mfma_f32_16x16x32_bf16 v[88:91], v[44:47], v[80:83], v[88:91]\n"
  "ds_read_b32 v146, v23 offset:4\n"
  "ds_read_b64 v[144:145], v22 offset:15360\n"
  "ds_read_b128 v[132:135], v21 offset:15360\n"
  "ds_read_b128 v[136:139], v21 offset:16384\n"
  "ds_read_b128 v[140:143], v21 offset:17408\n"
  "v_mfma_f32_16x16x32_bf16 v[92:95], v[48:51], v[72:75], 0\n"
  "v_fmac_f32 v125, v85, v126\n"
  "v_mfma_f32_16x16x32_bf16 v[92:95], v[52:55], v[76:79], v[92:95]\n"
  "v_mfma_f32_16x16x32_bf16 v[92:95], v[56:59], v[80:83], v[92:95]\n"
  "v_pk_fma_f32 v[100:101], v[88:89], v[126:127], v[100:101] op_sel_hi:[1,0,1]\n"
  "v_pk_fma_f32 v[102:103], v[90:91], v[126:127], v[102:103] op_sel_hi:[1,0,1]\n"
  "v_mfma_f32_16x16x32_bf16 v[96:99], v[60:63], v[72:75], 0\n"
  "v_mfma_f32_16x16x32_bf16 v[96:99], v[64:67], v[76:79], v[96:99]\n"
  "v_mfma_f32_16x16x32_bf16 v[96:99], v[68:71], v[80:83], v[96:99]\n"
  "s_nop 2\n"
  "v_pk_fma_f32 v[104:105], v[92:93], v[126:127], v[104:105] op_sel_hi:[1,0,1]\n"
  "v_pk_fma_f32 v[106:107], v[94:95], v[126:127], v[106:107] op_sel_hi:[1,0,1]\n"
  "s_waitcnt lgkmcnt(4)\n"
  "v_cmp_ne_u32 vcc, 1, v146\n"
  "s_nop 0\n"
  "v_pk_fma_f32 v[108:109], v[96:97], v[126:127], v[108:109] op_sel_hi:[1,0,1]\n"
  "v_pk_fma_f32 v[110:111], v[98:99], v[126:127], v[110:111] op_sel_hi:[1,0,1]\n"
  "s_cbranch_vccnz .Ldino_hwait1\n"
  ".Ldino_hgo1:\n"
  "s_waitcnt lgkmcnt(3)\n"
  "v_sub_f32 v126, v144, v124\n"
  "v_cmp_lt_f32 vcc, s50, v126\n"
  "s_waitcnt lgkmcnt(2)\n"
  "v_mfma_f32_16x16x32_bf16 v[88:91], v[36:39], v[132:135], 0\n"
  "s_cbranch_vccnz .Ldino_hrare1\n"
  ".Ldino_hback1:\n"
  "v_exp_f32 v126, v126\n"
  "s_waitcnt lgkmcnt(1)\n"
  "v_mfma_f32_16x16x32_bf16 v[88:91], v[40:43], v[136:139], v[88:91]\n"
  "s_waitcnt lgkmcnt(0)\n"
  "v_mfma_f32_16x16x32_bf16 v[88:91], v[44:47], v[140:143], v[88:91]\n"
  "s_cmp_eq_u32 s35, 1\n"
  "s_cbranch_scc1 .Ldino_hlast\n"
  "ds_read_b32 v86, v23 offset:8\n"
  "ds_read_b64 v[84:85], v22 offset:30720\n"
  "ds_read_b128 v[72:75], v21 offset:30720\n"
  "ds_read_b128 v[76:79], v21 offset:31744\n"
  "ds_read_b128 v[80:83], v21 offset:32768\n"
  "v_mfma_f32_16x16x32_bf16 v[92:95], v[48:51], v[132:135], 0\n"
  "v_fmac_f32 v125, v145, v126\n"
  "v_mfma_f32_16x16x32_bf16 v[92:95], v[52:55], v[136:139], v[92:95]\n"
  "v_mfma_f32_16x16x32_bf16 v[92:95], v[56:59], v[140:143], v[92:95]\n"
  "v_pk_fma_f32 v[100:101], v[88:89], v[126:127], v[100:101] op_sel_hi:[1,0,1]\n"
  "v_pk_fma_f32 v[102:103], v[90:91], v[126:127], v[102:103] op_sel_hi:[1,0,1]\n"
  "v_mfma_f32_16x16x32_bf16 v[96:99], v[60:63], v[132:135], 0\n"
  "v_mfma_f32_16x16x32_bf16 v[96:99], v[64:67], v[136:139], v[96:99]\n"
  "v_mfma_f32_16x16x32_bf16 v[96:99], v[68:71], v[140:143], v[96:99]\n"
  "s_nop 2\n"
  "v_pk_fma_f32 v[104:105], v[92:93], v[126:127], v[104:105] op_sel_hi:[1,0,1]\n"
  "v_pk_fma_f32 v[106:107], v[94:95], v[126:127], v[106:107] op_sel_hi:[1,0,1]\n"
  "v_add_u32 v21, 30720, v21\n"
  "v_add_u32 v22, 30720, v22\n"
  "v_add_u32 v23, 8, v23\n"
  "s_mov_b32 s35, 1\n"
  "s_waitcnt lgkmcnt(4)\n"
  "v_cmp_ne_u32 vcc, 1, v86\n"
  "v_pk_fma_f32 v[108:109], v[96:97], v[126:127], v[108:109] op_sel_hi:[1,0,1]\n"
  "v_pk_fma_f32 v[110:111], v[98:99], v[126:127], v[110:111] op_sel_hi:[1,0,1]\n"
  "s_cbranch_vccz .Ldino_hp\n"
  "s_setprio 0\n"
  ".Ldino_w_39:\n"
  "ds_read_b32 v14, v23 offset:0\n"
  "s_sleep 1\n"
  "s_waitcnt lgkmcnt(0)\n"
  "v_cmp_ne_u32 vcc, 1, v14\n"
  "s_cbranch_vccnz .Ldino_w_39\n"
  "s_setprio 2\n"
  "ds_read_b32 v86, v23 offset:0\n"
  "ds_read_b64 v[84:85], v22 offset:0\n"
  "ds_read_b128 v[72:75], v21 offset:0\n"
  "ds_read_b128 v[76:79], v21 offset:1024\n"
  "ds_read_b128 v[80:83], v21 offset:2048\n"
  "s_branch .Ldino_hp\n"
  ".Ldino_hwait1:\n"
  "s_cmp_eq_u32 s35, 1\n"
  "s_cbranch_scc1 .Ldino_hspin\n"
  "s_setprio 0\n"
  ".Ldino_w_41:\n"
  "ds_read_b32 v14, v23 offset:4\n"
  "s_sleep 1\n"
  "s_waitcnt lgkmcnt(0)\n"
  "v_cmp_ne_u32 vcc, 1, v14\n"
  "s_cbranch_vccnz .Ldino_w_41\n"
  "s_setprio 2\n"
  "ds_read_b32 v146, v23 offset:4\n"
  "ds_read_b64 v[144:145], v22 offset:15360\n"
  "ds_read_b128 v[132:135], v21 offset:15360\n"
  "ds_read_b128 v[136:139], v21 offset:16384\n"
  "ds_read_b128 v[140:143], v21 offset:17408\n"
  "s_branch .Ldino_hgo1\n"
  ".Ldino_hspin:\n"
  ".Ldino_hspin1:\n"
  "ds_read_b32 v146, v23 offset:4\n"
  "s_waitcnt lgkmcnt(4)\n"
  "ds_read_b64 v[144:145], v22 offset:15360\n"
  "s_waitcnt lgkmcnt(4)\n"
  "ds_read_b128 v[132:135], v21 offset:15360\n"
  "s_waitcnt lgkmcnt(4)\n"
  "ds_read_b128 v[136:139], v21 offset:16384\n"
  "s_waitcnt lgkmcnt(4)\n"
  "ds_read_b128 v[140:143], v21 offset:17408\n"
  "s_waitcnt lgkmcnt(4)\n"
  "v_cmp_ne_u32 vcc, 1, v146\n"
  "s_cbranch_vccz .Ldino_hgo1\n"
  "s_branch .Ldino_hspin1\n"
  ".Ldino_hrare0:\n"
  "v_max_f32 v126, 0, v126\n"
  "v_exp_f32 v127, -v126\n"
  "v_add_f32 v124, v124, v126\n"
  "v_mul_f32 v100, v100, v127\n"
  "v_mul_f32 v101, v101, v127\n"
  "v_mul_f32 v102, v102, v127\n"
  "v_mul_f32 v103, v103, v127\n"
  "v_mul_f32 v104, v104, v127\n"
  "v_mul_f32 v105, v105, v127\n"
  "v_mul_f32 v106, v106, v127\n"
  "v_mul_f32 v107, v107, v127\n"
  "v_mul_f32 v108, v108, v127\n"
  "v_mul_f32 v109, v109, v127\n"
  "v_mul_f32 v110, v110, v127\n"
  "v_mul_f32 v111, v111, v127\n"
  "v_mul_f32 v125, v125, v127\n"
  "v_sub_f32 v126, v84, v124\n"
  "s_branch .Ldino_hback0\n"
  ".Ldino_hrare1:\n"
  "v_max_f32 v126, 0, v126\n"
  "v_exp_f32 v127, -v126\n"
  "v_add_f32 v124, v124, v126\n"
  "v_mul_f32 v100, v100, v127\n"
  "v_mul_f32 v101, v101, v127\n"
  "v_mul_f32 v102, v102, v127\n"
  "v_mul_f32 v103, v103, v127\n"
  "v_mul_f32 v104, v104, v127\n"
  "v_mul_f32 v105, v105, v127\n"
  "v_mul_f32 v106, v106, v127\n"
  "v_mul_f32 v107, v107, v127\n"
  "v_mul_f32 v108, v108, v127\n"
  "v_mul_f32 v109, v109, v127\n"
  "v_mul_f32 v110, v110, v127\n"
  "v_mul_f32 v111, v111, v127\n"
  "v_mul_f32 v125, v125, v127\n"
  "v_sub_f32 v126, v144, v124\n"
  "s_branch .Ldino_hback1\n"
  ".Ldino_hlast:\n"
  "v_mfma_f32_16x16x32_bf16 v[92:95], v[48:51], v[132:135], 0\n"
  "v_fmac_f32 v125, v145, v126\n"
  "v_and_or_b32 v113, v124, s47, v24\n"
  "v_mfma_f32_16x16x32_bf16 v[92:95], v[52:55], v[136:139], v[92:95]\n"
  "v_mul_f32 v125, 0x3c2aaaab, v125\n"
  "v_lshl_or_b32 v114, v124, 16, v24\n"
  "v_mfma_f32_16x16x32_bf16 v[92:95], v[56:59], v[140:143], v[92:95]\n"
  "v_and_or_b32 v112, v125, s46, v24\n"
  "v_mov_b32 v118, v114\n"
  "v_mfma_f32_16x16x32_bf16 v[96:99], v[60:63], v[132:135], 0\n"
  "v_mov_b32 v122, v114\n"
  "v_mov_b64 v[116:117], v[112:113]\n"
  "v_mfma_f32_16x16x32_bf16 v[96:99], v[64:67], v[136:139], v[96:99]\n"
  "v_mov_b64 v[120:121], v[112:113]\n"
  "s_mov_b64 exec, s[48:49]\n"
  "v_mfma_f32_16x16x32_bf16 v[96:99], v[68:71], v[140:143], v[96:99]\n"
  "v_pk_fma_f32 v[100:101], v[88:89], v[126:127], v[100:101] op_sel_hi:[1,0,1]\n"
  "v_pk_fma_f32 v[102:103], v[90:91], v[126:127], v[102:103] op_sel_hi:[1,0,1]\n"
  "v_pk_fma_f32 v[104:105], v[92:93], v[126:127], v[104:105] op_sel_hi:[1,0,1]\n"
  "v_pk_fma_f32 v[106:107], v[94:95], v[126:127], v[106:107] op_sel_hi:[1,0,1]\n"
  "v_and_or_b32 v112, v100, s46, v24\n"
  "v_and_or_b32 v113, v101, s46, v24\n"
  "v_and_or_b32 v114, v102, s46, v24\n"
  "v_and_or_b32 v115, v103, s46, v24\n"
  "v_and_or_b32 v116, v104, s46, v24\n"
  "v_and_or_b32 v117, v105, s46, v24\n"
  "v_and_or_b32 v118, v106, s46, v24\n"
  "v_and_or_b32 v119, v107, s46, v24\n"
  "v_pk_fma_f32 v[108:109], v[96:97], v[126:127], v[108:109] op_sel_hi:[1,0,1]\n"
  "v_pk_fma_f32 v[110:111], v[98:99], v[126:127], v[110:111] op_sel_hi:[1,0,1]\n"
  "v_and_or_b32 v120, v108, s46, v24\n"
  "v_and_or_b32 v121, v109, s46, v24\n"
  "v_and_or_b32 v122, v110, s46, v24\n"
  "v_and_or_b32 v123, v111, s46, v24\n"
  "s_mov_b64 exec, -1\n"
  "buffer_store_dwordx4 v[112:115], v33, s[24:27], 0 offen sc1\n"
  "buffer_store_dwordx4 v[116:119], v33, s[24:27], 0 offen offset:128 sc1\n"
  "buffer_store_dwordx4 v[120:123], v34, s[24:27], 0 offen sc1\n"
  "s_branch .Ldino_red\n"
  : "+{v0}"(tid)
  : "{s[0:1]}"(__builtin_amdgcn_kernarg_segment_ptr()), "{s2}"(blockIdx.x)
  : "v1", "v2", "v3", "v4", "v5", "v6", "v7", "v8", "v9", "v10", "v11", "v12", "v13", "v14", "v15", "v16", "v17", "v18", "v19", "v20", "v21", "v22", "v23", "v24", "v25", "v26", "v27", "v28", "v29", "v30", "v31", "v32", "v33", "v34", "v35", "v36", "v37", "v38", "v39", "v40", "v41", "v42", "v43", "v44", "v45", "v46", "v47", "v48", "v49", "v50", "v51", "v52", "v53", "v54", "v55", "v56", "v57", "v58", "v59", "v60", "v61", "v62", "v63", "v64", "v65", "v66", "v67", "v68", "v69", "v70", "v71", "v72", "v73", "v74", "v75", "v76", "v77", "v78", "v79", "v80", "v81", "v82", "v83", "v84", "v85", "v86", "v87", "v88", "v89", "v90", "v91", "v92", "v93", "v94", "v95", "v96", "v97", "v98", "v99", "v100", "v101", "v102", "v103", "v104", "v105", "v106", "v107", "v108", "v109", "v110", "v111", "v112", "v113", "v114", "v115", "v116", "v117", "v118", "v119", "v120", "v121", "v122", "v123", "v124", "v125", "v126", "v127", "v128", "v129", "v130", "v131", "v132", "v133", "v134", "v135", "v136", "v137", "v138", "v139", "v140", "v141", "v142", "v143", "v144", "v145", "v146", "v147", "v148", "v149", "v150", "v151", "v152", "v153", "v154", "v155", "v156", "v157", "v158", "v159", "v160", "v161", "v162", "v163", "v164", "v165", "v166", "v167", "v168", "v169", "v170", "v171", "v172", "v173", "v174", "v175", "v176", "v177", "v178", "v179", "v180", "v181", "v182", "v183", "v184", "v185", "v186", "v187", "v188", "v189", "v190", "v191", "v192", "v193", "v194", "v195", "v196", "v197", "v198", "v199", "v200", "v201", "v202", "v203", "v204", "v205", "v206", "v207", "v208", "v209", "v210", "v211", "v212", "v213", "v214", "v215", "v216", "v217", "v218", "v219", "v220", "v221", "v222", "v223", "v224", "v225", "v226", "v227", "v228", "v229", "v230", "v231", "v232", "v233", "v234", "v235", "v236", "v237", "v238", "v239", "v240", "v241", "v242", "v243", "v244", "v245", "v246", "v247", "v248", "v249", "v250", "v251", "v252", "v253", "v254", "s3", "s4", "s5", "s6", "s7", "s8", "s9", "s10", "s11", "s12", "s13", "s14", "s15", "s16", "s17", "s18", "s19", "s20", "s21", "s22", "s23", "s24", "s25", "s26", "s27", "s28", "s29", "s30", "s31", "s32", "s33", "s34", "s35", "s36", "s37", "s38", "s39", "s40", "s41", "s42", "s43", "s44", "s45", "s46", "s47", "s48", "s49", "s50", "s51", "s52", "s53", "s54", "s55", "s56", "s57", "s58", "s59", "vcc", "memory");
}


extern "C" void kernel_launch(void* const* d_in, const int* in_sizes, int n_in, void* d_out, int out_size, void* d_ws, size_t ws_size,
                              hipStream_t stream) {
  (void)hipFuncSetAttribute((const void*)dino_kernel, hipFuncAttributeMaxDynamicSharedMemorySize, 163328);
  dino_kernel<<<256, 512, 163328, stream>>>(d_in[0], d_in[1], d_in[2], d_in[3], d_in[4], d_in[5], d_in[6], d_out, d_ws);
}
```
